```python
import torch

def _softmax(x, axis=-1):
    return torch.softmax(x, dim=axis)
def _gelu(x):
    return torch.nn.functional.gelu(x, approximate="tanh")
def _silu(x):
    return torch.nn.functional.silu(x)
def _sigmoid(x):
    return torch.sigmoid(x)
def _layernorm(x, axis=-1, eps=1e-5):
    m = x.mean(dim=axis, keepdim=True)
    v = x.var(dim=axis, keepdim=True, unbiased=False)
    return (x - m) / torch.sqrt(v + eps)
def _scatter_add(tgt, idx, src, axis=0):
    return tgt.clone().index_add_(axis, idx, src)
def _segment_sum(src, seg, n):
    out = torch.zeros((n,) + src.shape[1:], dtype=src.dtype)
    return out.index_add_(0, seg, src)
def _pad(x, pw):
    flat = tuple(v for pair in reversed(pw) for v in pair)
    return torch.nn.functional.pad(x, flat)

kernel_name = 'parallel_sum__ssm_conv_s4__dense_activation_relu__prompt'

def setup_inputs():

    _w = (lambda a: (lambda t: t.to(torch.bfloat16) if t.is_floating_point() else t)(torch.as_tensor(a)))
    def _A_setup():
        B, L, D, N = 1, 2048, 576, 128
        A_re = -(lambda _x: torch.exp(torch.as_tensor(_x)))((lambda *s: torch.randn(*s, dtype=torch.bfloat16) * 3.2)(D, N))
        A_im = torch.pi * (lambda *s: torch.randn(*s, dtype=torch.bfloat16) * 0.2)(D, N)
        B_re = (lambda *s: torch.randn(*s, dtype=torch.bfloat16) * 0.4)(D, N)
        B_im = (lambda *s: torch.randn(*s, dtype=torch.bfloat16) * 0.4)(D, N)
        C_re = (lambda *s: torch.randn(*s, dtype=torch.bfloat16) * 0.4)(D, N)
        C_im = (lambda *s: torch.randn(*s, dtype=torch.bfloat16) * 0.4)(D, N)
        log_dt = (lambda *s: torch.randn(*s, dtype=torch.bfloat16) * 0.2)(D) * 0.5 - 4.0
        Dskip = (lambda *s: torch.randn(*s, dtype=torch.bfloat16) * 0.4)(D)
        return {
            "u": _w((lambda *s: torch.randn(*s, dtype=torch.bfloat16) * 0.05)(B, L, D)),
            "A_re": _w(A_re),
            "A_im": _w(A_im),
            "B_re": _w(B_re),
            "B_im": _w(B_im),
            "C_re": _w(C_re),
            "C_im": _w(C_im),
            "log_dt": _w(log_dt),
            "Dskip": _w(Dskip),
        }
    _A_in = _A_setup()

    def _B_setup():
        B, S, D, Dff = 1, 2048, 576, 4608
        return {
            "x": _w((lambda *s: torch.randn(*s, dtype=torch.bfloat16) * 0.05)(B, S, D)),
            "w1": _w((lambda *s: torch.randn(*s, dtype=torch.bfloat16) * 0.05)(D, Dff)),
            "w2": _w((lambda *s: torch.randn(*s, dtype=torch.bfloat16) * 0.05)(Dff, D)),
        }
    _B_in = _B_setup()

    return {
        "A_u": _A_in["u"],
        "A_A_re": _A_in["A_re"],
        "A_A_im": _A_in["A_im"],
        "A_B_re": _A_in["B_re"],
        "A_B_im": _A_in["B_im"],
        "A_C_re": _A_in["C_re"],
        "A_C_im": _A_in["C_im"],
        "A_log_dt": _A_in["log_dt"],
        "A_Dskip": _A_in["Dskip"],
        "B_x": _B_in["x"],
        "B_w1": _B_in["w1"],
        "B_w2": _B_in["w2"],
    }

def reference(A_u, A_A_re, A_A_im, A_B_re, A_B_im, A_C_re, A_C_im, A_log_dt, A_Dskip, B_x, B_w1, B_w2):
    def _A_ref(u, A_re, A_im, B_re, B_im, C_re, C_im, log_dt, Dskip):
        Bsz, L, D = u.shape
        dt = torch.exp(log_dt)[:, None]
        A = A_re + 1j * A_im
        Bc = B_re + 1j * B_im
        Cc = C_re + 1j * C_im
        logdA = dt * A
        dB = dt * Bc
        l = (lambda x, dt: x.to(dt))(torch.arange(L), A_re.dtype)
        P = torch.exp(logdA[:, :, None] * l[None, None, :])
        K = torch.einsum("dn,dnl,dn->dl", Cc, P, dB).real
        L2 = 2 * L
        Kp = _pad(K, ((0, 0), (0, L)))
        up = (lambda x, *d: x.permute(*d))(u, 0, 2, 1)
        up = _pad(up, ((0, 0), (0, 0), (0, L)))
        Kf = (lambda x, axes=(-2,-1): torch.fft.rfft2(x.float(), dim=axes))(Kp[:, None, :], axes=(-2, -1))
        Uf = (lambda x, axes=(-2,-1): torch.fft.rfft2(x.float(), dim=axes))(up[:, :, None, :], axes=(-2, -1))
        Yf = Uf * Kf[None, :, :, :]
        y = (lambda x, s, axes=(-2,-1): torch.fft.irfft2(x, s=s, dim=axes).to(torch.bfloat16))(Yf, (1, L2), axes=(-2, -1))[:, :, 0, :L]
        y = (lambda x, *d: x.permute(*d))(y, 0, 2, 1) + u * Dskip[None, None, :]
        return y

    def _B_ref(x, w1, w2):
        h = torch.einsum("bsd,df->bsf", x, w1)
        zero = torch.zeros_like(h)
        h = torch.maximum(h, zero)
        return torch.einsum("bsf,fd->bsd", h, w2)

    x = A_u
    xn = _layernorm(x, axis=-1)
    return x + _A_ref(xn, A_A_re, A_A_im, A_B_re, A_B_im, A_C_re, A_C_im, A_log_dt, A_Dskip) + _B_ref(xn, B_w1, B_w2)
```

```cpp
#include <hip/hip_runtime.h>
#include <hip/hip_bf16.h>
#include <stdint.h>
typedef unsigned short u16;
constexpr int L=2048, D=576, N=128, F=4608;
__device__ __forceinline__ float bf2f(u16 v){ return __uint_as_float(((unsigned)v)<<16); }
__device__ __forceinline__ u16 f2bf(float f){ unsigned u=__float_as_uint(f); u += 0x7fffu + ((u>>16)&1u); return (u16)(u>>16); }
__device__ __forceinline__ float rbf(float f){ return bf2f(f2bf(f)); }

__global__ void ln_k(const u16* x, u16* xn){
  int row=blockIdx.x; int lane=threadIdx.x;
  float v[9]; float s=0;
  for(int i=0;i<9;i++){ v[i]=bf2f(x[row*D+lane+64*i]); s+=v[i]; }
  for(int o=32;o;o>>=1) s+=__shfl_xor(s,o);
  float mean=s/D; float q=0;
  for(int i=0;i<9;i++){ float d=v[i]-mean; q+=d*d; }
  for(int o=32;o;o>>=1) q+=__shfl_xor(q,o);
  float var=rbf(q/D); float m=rbf(mean);
  float den=rbf(sqrtf(rbf(var+1e-5f)));
  for(int i=0;i<9;i++){ xn[row*D+lane+64*i]=f2bf(rbf(v[i]-m)/den); }
}
__global__ void kgen_k(const u16* Are,const u16* Aim,const u16* Bre,const u16* Bim,const u16* Cre,const u16* Cim,const u16* ldt,float* K){
  int d=blockIdx.x;
  __shared__ float ar[N], ai[N], cr[N], ci[N];
  float dt=rbf(expf(bf2f(ldt[d])));
  if(threadIdx.x<N){ int n=threadIdx.x;
    ar[n]=dt*bf2f(Are[d*N+n]); ai[n]=dt*bf2f(Aim[d*N+n]);
    float br=dt*bf2f(Bre[d*N+n]), bi=dt*bf2f(Bim[d*N+n]);
    float Cr=bf2f(Cre[d*N+n]), Ci=bf2f(Cim[d*N+n]);
    cr[n]=Cr*br-Ci*bi; ci[n]=Cr*bi+Ci*br; }
  __syncthreads();
  for(int l=threadIdx.x;l<L;l+=blockDim.x){
    float lf=rbf((float)l); float s=0;
    for(int n=0;n<N;n++){ float e=expf(ar[n]*lf); float sn,cs; sincosf(ai[n]*lf,&sn,&cs); s+=e*(cr[n]*cs-ci[n]*sn); }
    K[d*L+l]=s;
  }
}
__global__ void conv_k(const u16* xn,const float* K,float* y){
  int d=blockIdx.x; __shared__ float u[L], k[L];
  for(int t=threadIdx.x;t<L;t+=blockDim.x){ u[t]=bf2f(xn[t*D+d]); k[t]=K[d*L+t]; }
  __syncthreads();
  for(int t=threadIdx.x;t<L;t+=blockDim.x){ float s=0; for(int j=0;j<=t;j++) s+=k[j]*u[t-j]; y[t*D+d]=s; }
}
__global__ void g1_k(const u16* xn,const u16* w1,u16* h){
  int f=blockIdx.x*256+threadIdx.x; int t0=blockIdx.y*8; float s[8]={};
  for(int k=0;k<D;k++){ float w=bf2f(w1[k*F+f]); for(int i=0;i<8;i++) s[i]+=bf2f(xn[(t0+i)*D+k])*w; }
  for(int i=0;i<8;i++) h[(t0+i)*F+f]=f2bf(fmaxf(s[i],0.f));
}
__global__ void g2_k(const u16* h,const u16* w2,const u16* x,const u16* xn,const float* y,const u16* Dk,u16* out){
  int d=blockIdx.x*64+threadIdx.x; int t0=blockIdx.y*4; float s[4]={};
  for(int k=0;k<F;k++){ float w=bf2f(w2[k*D+d]); for(int i=0;i<4;i++) s[i]+=bf2f(h[(t0+i)*F+k])*w; }
  for(int i=0;i<4;i++){ int idx=(t0+i)*D+d;
    float a=rbf(rbf(y[idx])+rbf(bf2f(xn[idx])*bf2f(Dk[d])));
    float r=rbf(bf2f(x[idx])+a);
    out[idx]=f2bf(r+rbf(s[i])); }
}
extern "C" void kernel_launch(void* const* d_in, const int* in_sizes, int n_in, void* d_out, int out_size, void* d_ws, size_t ws_size, hipStream_t stream){
  const u16* x=(const u16*)d_in[0];
  char* ws=(char*)d_ws;
  u16* xn=(u16*)ws; float* K=(float*)(ws+(4<<20)); float* y=(float*)(ws+(16<<20)); u16* h=(u16*)(ws+(32<<20));
  ln_k<<<L,64,0,stream>>>(x,xn);
  kgen_k<<<D,256,0,stream>>>((const u16*)d_in[1],(const u16*)d_in[2],(const u16*)d_in[3],(const u16*)d_in[4],(const u16*)d_in[5],(const u16*)d_in[6],(const u16*)d_in[7],K);
  conv_k<<<D,256,0,stream>>>(xn,K,y);
  g1_k<<<dim3(F/256,L/8),256,0,stream>>>(xn,(const u16*)d_in[10],h);
  g2_k<<<dim3(D/64,L/4),64,0,stream>>>(h,(const u16*)d_in[11],x,xn,y,(const u16*)d_in[8],(u16*)d_out);
}
```

```cpp
#include <hip/hip_runtime.h>
#include <stdint.h>
#define CLOB_SSM "v0","v1","v2","v3","v4","v5","v6","v7","v16","v17","v18","v19","v20","v21","v22","v23","v24","v25","v26","v27","v28","v29","v30","v31","v32","v33","v34","v35","v36","v37","v38","v39","v40","v41","v42","v43","v44","v45","v46","v47","v104","v105","v106","v107","v108","v109","v110","v111","v112","v113","v114","v115","v116","v117","v118","v119","v120","v121","v122","v123","v124","v125","v126","v127","v128","v129","v130","v131","v132","v133","v134","v135","v136","v137","v138","v139","v140","v141","v142","v143","v144","v145","v146","v147","v148","v149","v150","v151","v152","v153","v154","v155","v156","v157","v158","v159","v160","v161","v162","v163","v164","v165","v166","v167","v168","v169","v170","v171","v172","v173","v174","v175","v176","v177","v178","v179","v180","v181","v182","v183","v184","v185","v186","v187","v188","v189","v190","v191","v192","v193","v194","v195","v196","v197","v198","v199","v200","v201","v202","v203","v204","v205","v206","v207","v208","v209","v210","v211","v212","v213","v214","v215","v216","v217","v218","v219","v220","v221","v222","v223","v224","v225","v226","v227","v228","v229","v230","v231","v232","v233","v234","v235","v236","v237","v238","v239","v240","v241","v242","v243","v244","v245","v246","v247","v248","v249","v250","v251","v252","v253","v254","v255","a0","a1","a2","a3","a4","a5","a6","a7","a8","a9","a10","a11","a12","a13","a14","a15","a16","a17","a18","a19","a20","a21","a22","a23","a24","a25","a26","a27","a28","a29","a30","a31","a32","a33","a34","a35","a36","a37","a38","a39","a40","a41","a42","a43","a44","a45","a46","a47","a48","a49","a50","a51","a52","a53","a54","a55","a56","a57","a58","a59","a60","a61","a62","a63","a64","a65","a66","a67","a68","a69","a70","a71","a72","a73","a74","a75","a76","a77","a78","a79","a80","a81","a82","a83","a84","a85","a86","a87","a88","a89","a90","a91","a92","a93","a94","a95","a96","a97","a98","a99","a100","a101","a102","a103","a104","a105","a106","a107","a108","a109","a110","a111","a112","a113","a114","a115","a116","a117","a118","a119","a120","a121","a122","a123","a124","a125","a126","a127","a128","a129","a130","a131","a132","a133","a134","a135","a136","a137","a138","a139","a140","a141","a142","a143","a144","a145","a146","a147","a148","a149","a150","a151","a152","a153","a154","a155","a156","a157","a158","a159","a160","a161","a162","a163","a164","a165","a166","a167","a168","a169","a170","a171","a172","a173","a174","a175","a176","a177","a178","a179","a180","a181","a182","a183","a184","a185","a186","a187","a188","a189","a190","a191","a192","a193","a194","a195","a196","a197","a198","a199","a200","a201","a202","a203","a204","a205","a206","a207","a208","a209","a210","a211","a212","a213","a214","a215","a216","a217","a218","a219","a220","a221","a222","a223","a224","a225","a226","a227","a228","a229","a230","a231","a232","a233","a234","a235","a236","a237","a238","a239","a240","a241","a242","a243","a244","a245","a246","a247","a248","a249","a250","s40","s41","s42","s43","s44","s45","s46","s47","s48","s49","s50","s51","s52","s53","s54","s55","s56","s57","s58","s59","s60","s61","s62","s63","s64","s65","s66","s67","s68","s69","s70","s71","s72","s73","s74","s75","s76","s77","s78","s79","s80","s81","s82","s83","s84","s85","s86","s87","s88","s89","s90","s91","s92","s93","s94","s95"
#define CLOB_ALL "v0","v1","v2","v3","v4","v5","v6","v7","v8","v9","v10","v11","v13","v14","v15","v16","v17","v18","v19","v20","v21","v22","v23","v24","v25","v26","v27","v28","v29","v30","v31","v32","v33","v34","v35","v36","v37","v38","v39","v40","v41","v42","v43","v44","v45","v46","v47","v48","v49","v50","v51","v52","v53","v54","v55","v56","v57","v58","v59","v60","v61","v62","v63","v64","v65","v66","v67","v68","v69","v70","v71","v72","v73","v74","v75","v76","v77","v78","v79","v80","v81","v82","v83","v84","v85","v86","v87","v88","v89","v90","v91","v92","v93","v94","v95","v96","v97","v98","v99","v100","v101","v102","v103","v104","v105","v106","v107","v108","v109","v110","v111","v112","v113","v114","v115","v116","v117","v118","v119","v120","v121","v122","v123","v124","v125","v126","v127","v128","v129","v130","v131","v132","v133","v134","v135","v136","v137","v138","v139","v140","v141","v142","v143","v144","v145","v146","v147","v148","v149","v150","v151","v152","v153","v154","v155","v156","v157","v158","v159","v160","v161","v162","v163","v164","v165","v166","v167","v168","v169","v170","v171","v172","v173","v174","v175","v176","v177","v178","v179","v180","v181","v182","v183","v184","v185","v186","v187","v188","v189","v190","v191","v192","v193","v194","v195","v196","v197","v198","v199","v200","v201","v202","v203","v204","v205","v206","v207","v208","v209","v210","v211","v212","v213","v214","v215","v216","v217","v218","v219","v220","v221","v222","v223","v224","v225","v226","v227","v228","v229","v230","v231","v232","v233","v234","v235","v236","v237","v238","v239","v240","v241","v242","v243","v244","v245","v246","v247","v248","v249","v250","v251","v252","v253","v254","v255","a0","a1","a2","a3","a4","a5","a6","a7","a8","a9","a10","a11","a12","a13","a14","a15","a16","a17","a18","a19","a20","a21","a22","a23","a24","a25","a26","a27","a28","a29","a30","a31","a32","a33","a34","a35","a36","a37","a38","a39","a40","a41","a42","a43","a44","a45","a46","a47","a48","a49","a50","a51","a52","a53","a54","a55","a56","a57","a58","a59","a60","a61","a62","a63","a64","a65","a66","a67","a68","a69","a70","a71","a72","a73","a74","a75","a76","a77","a78","a79","a80","a81","a82","a83","a84","a85","a86","a87","a88","a89","a90","a91","a92","a93","a94","a95","a96","a97","a98","a99","a100","a101","a102","a103","a104","a105","a106","a107","a108","a109","a110","a111","a112","a113","a114","a115","a116","a117","a118","a119","a120","a121","a122","a123","a124","a125","a126","a127","s40","s41","s42","s43","s44","s45","s46","s47","s48","s49","s50","s51","s52","s53","s54","s55","s56","s57","s58","s59","s60","s61","s62","s63","s64","s65","s66","s67","s68","s69","s70","s71","s72","s73","s74","s75","s76","s77","s78","s79","s80","s81","s82","s83","s84","s85","s86","s87","s88","s89","s90","s91","s92","s93","s94","s95","s96","s97","s98","s99"
#define FFN_EARLY \
"v_mbcnt_lo_u32_b32 v0, -1, 0\n" \
"v_mbcnt_hi_u32_b32 v0, -1, v0\n" \
"v_lshrrev_b32 v48, 3, v0\n" \
"v_and_b32 v13, 7, v0\n" \
"v_lshrrev_b32 v2, 4, v0\n" \
"v_xor_b32 v2, v13, v2\n" \
"v_lshlrev_b32 v2, 4, v2\n" \
"v_mul_u32_u24 v3, 1152, v48\n" \
"v_add_u32 v1, v3, v2\n" \
"v_xor_b32 v2, 64, v2\n" \
"v_add_u32 v11, v3, v2\n" \
"s_mul_i32 s46, s45, 36864\n" \
"s_and_b32 s41, s41, 0xffff\n" \
"s_mov_b32 s42, 36864\n" \
"s_mov_b32 s43, 0x20000\n" \
"s_add_u32 m0, s46, 0\n" \
"s_mov_b32 s44, 0\n" \
"buffer_load_dwordx4 v1, s[40:43], s44 offen lds\n" \
"s_add_u32 m0, s46, 1024\n" \
"s_mov_b32 s44, 9216\n" \
"buffer_load_dwordx4 v11, s[40:43], s44 offen lds\n" \
"s_add_u32 m0, s46, 2048\n" \
"s_mov_b32 s44, 18432\n" \
"buffer_load_dwordx4 v1, s[40:43], s44 offen lds\n" \
"s_add_u32 m0, s46, 3072\n" \
"s_mov_b32 s44, 27648\n" \
"buffer_load_dwordx4 v11, s[40:43], s44 offen lds\n" \
"s_add_u32 m0, s46, 4096\n" \
"s_mov_b32 s44, 128\n" \
"buffer_load_dwordx4 v1, s[40:43], s44 offen lds\n" \
"s_add_u32 m0, s46, 5120\n" \
"s_mov_b32 s44, 9344\n" \
"buffer_load_dwordx4 v11, s[40:43], s44 offen lds\n" \
"s_add_u32 m0, s46, 6144\n" \
"s_mov_b32 s44, 18560\n" \
"buffer_load_dwordx4 v1, s[40:43], s44 offen lds\n" \
"s_add_u32 m0, s46, 7168\n" \
"s_mov_b32 s44, 27776\n" \
"buffer_load_dwordx4 v11, s[40:43], s44 offen lds\n" \
"s_add_u32 m0, s46, 8192\n" \
"s_mov_b32 s44, 256\n" \
"buffer_load_dwordx4 v1, s[40:43], s44 offen lds\n" \
"s_add_u32 m0, s46, 9216\n" \
"s_mov_b32 s44, 9472\n" \
"buffer_load_dwordx4 v11, s[40:43], s44 offen lds\n" \
"s_add_u32 m0, s46, 10240\n" \
"s_mov_b32 s44, 18688\n" \
"buffer_load_dwordx4 v1, s[40:43], s44 offen lds\n" \
"s_add_u32 m0, s46, 11264\n" \
"s_mov_b32 s44, 27904\n" \
"buffer_load_dwordx4 v11, s[40:43], s44 offen lds\n" \
""
#define FFN_BODY \
"s_and_b32 s41, s41, 0xffff\n" \
"s_mov_b32 s42, 36864\n" \
"s_mov_b32 s43, 0x20000\n" \
"s_mul_i32 s65, s61, 36864\n" \
"v_mbcnt_lo_u32_b32 v0, -1, 0\n" \
"v_mbcnt_hi_u32_b32 v0, -1, v0\n" \
"v_lshrrev_b32 v48, 3, v0\n" \
"v_and_b32 v13, 7, v0\n" \
"v_lshrrev_b32 v2, 4, v0\n" \
"v_xor_b32 v2, v13, v2\n" \
"v_lshlrev_b32 v2, 4, v2\n" \
"v_mul_u32_u24 v3, 1152, v48\n" \
"v_add_u32 v1, v3, v2\n" \
"v_xor_b32 v2, 64, v2\n" \
"v_add_u32 v11, v3, v2\n" \
"s_add_u32 m0, s65, 12288\n" \
"s_mov_b32 s59, 384\n" \
"buffer_load_dwordx4 v1, s[40:43], s59 offen lds\n" \
"s_and_b32 s45, s45, 0xffff\n" \
"s_mov_b32 s47, 0x20000\n" \
"s_and_b32 s49, s49, 0xffff\n" \
"s_and_b32 s53, s53, 0xffff\n" \
"s_mov_b32 s54, 1769472\n" \
"s_add_u32 m0, s65, 13312\n" \
"s_mov_b32 s59, 9600\n" \
"buffer_load_dwordx4 v11, s[40:43], s59 offen lds\n" \
"s_mov_b32 s55, 0x20000\n" \
"s_mul_i32 s56, s61, 36864\n" \
"s_lshl_b32 s58, s61, 10\n" \
"s_mul_i32 s57, s61, 4608\n" \
"s_mov_b32 s66, 294912\n" \
"s_add_u32 m0, s65, 14336\n" \
"s_mov_b32 s59, 18816\n" \
"buffer_load_dwordx4 v1, s[40:43], s59 offen lds\n" \
"s_mov_b32 s67, 0\n" \
"s_mov_b32 s68, 4289659008\n" \
"s_mov_b32 s69, s58\n" \
"s_add_u32 s70, s58, 73728\n" \
"s_and_b32 s71, s61, 1\n" \
"s_add_u32 m0, s65, 15360\n" \
"s_mov_b32 s59, 28032\n" \
"buffer_load_dwordx4 v11, s[40:43], s59 offen lds\n" \
"s_lshl_b32 s77, s71, 2\n" \
"s_mov_b32 s71, 73728\n" \
"s_mov_b32 s72, 1\n" \
"s_mov_b32 s73, 0\n" \
"s_mov_b32 s83, 0\n" \
"s_add_u32 m0, s65, 16384\n" \
"s_mov_b32 s59, 512\n" \
"buffer_load_dwordx4 v1, s[40:43], s59 offen lds\n" \
"v_lshrrev_b32 v2, 3, v0\n" \
"v_and_b32 v2, 2, v2\n" \
"v_xor_b32 v13, v13, v2\n" \
"v_xor_b32 v13, s77, v13\n" \
"v_lshlrev_b32 v13, 4, v13\n" \
"s_add_u32 m0, s65, 17408\n" \
"s_mov_b32 s59, 9728\n" \
"buffer_load_dwordx4 v11, s[40:43], s59 offen lds\n" \
"v_and_b32 v3, 3, v48\n" \
"v_lshrrev_b32 v48, 5, v0\n" \
"v_lshl_add_u32 v3, v48, 4, v3\n" \
"v_mul_u32_u24 v2, 9216, v3\n" \
"v_add_u32 v2, v2, v13\n" \
"s_add_u32 m0, s65, 18432\n" \
"s_mov_b32 s59, 18944\n" \
"buffer_load_dwordx4 v1, s[40:43], s59 offen lds\n" \
"v_mul_u32_u24 v3, 1152, v3\n" \
"v_add_u32 v3, v3, v13\n" \
"v_and_b32 v50, 15, v0\n" \
"v_lshrrev_b32 v51, 4, v0\n" \
"v_lshlrev_b32 v51, 3, v51\n" \
"s_add_u32 m0, s65, 19456\n" \
"s_mov_b32 s59, 28160\n" \
"buffer_load_dwordx4 v11, s[40:43], s59 offen lds\n" \
"v_sub_u32 v50, v50, v51\n" \
"v_and_b32 v51, 1, v50\n" \
"v_lshlrev_b32 v51, 4, v51\n" \
"v_mov_b32 v52, 0x3f80\n" \
"v_lshlrev_b32 v51, v51, v52\n" \
"s_add_u32 m0, s65, 20480\n" \
"s_mov_b32 s59, 640\n" \
"buffer_load_dwordx4 v1, s[40:43], s59 offen lds\n" \
"v_ashrrev_i32 v50, 1, v50\n" \
"v_cmp_eq_u32 vcc, 0, v50\n" \
"s_nop 1\n" \
"v_cndmask_b32 v52, 0, v51, vcc\n" \
"v_accvgpr_write_b32 a80, v52\n" \
"s_add_u32 m0, s65, 21504\n" \
"s_mov_b32 s59, 9856\n" \
"buffer_load_dwordx4 v11, s[40:43], s59 offen lds\n" \
"v_cmp_eq_u32 vcc, 1, v50\n" \
"s_nop 1\n" \
"v_cndmask_b32 v52, 0, v51, vcc\n" \
"v_accvgpr_write_b32 a81, v52\n" \
"v_cmp_eq_u32 vcc, 2, v50\n" \
"s_add_u32 m0, s65, 22528\n" \
"s_mov_b32 s59, 19072\n" \
"buffer_load_dwordx4 v1, s[40:43], s59 offen lds\n" \
"s_nop 1\n" \
"v_cndmask_b32 v52, 0, v51, vcc\n" \
"v_accvgpr_write_b32 a82, v52\n" \
"v_cmp_eq_u32 vcc, 3, v50\n" \
"s_nop 1\n" \
"s_add_u32 m0, s65, 23552\n" \
"s_mov_b32 s59, 28288\n" \
"buffer_load_dwordx4 v11, s[40:43], s59 offen lds\n" \
"v_cndmask_b32 v52, 0, v51, vcc\n" \
"v_accvgpr_write_b32 a83, v52\n" \
"v_cmp_eq_u32 vcc, -8, v50\n" \
"s_nop 1\n" \
"v_cndmask_b32 v52, 0, v51, vcc\n" \
"s_add_u32 m0, s65, 24576\n" \
"s_mov_b32 s59, 768\n" \
"buffer_load_dwordx4 v1, s[40:43], s59 offen lds\n" \
"v_accvgpr_write_b32 a84, v52\n" \
"v_cmp_eq_u32 vcc, -7, v50\n" \
"s_nop 1\n" \
"v_cndmask_b32 v52, 0, v51, vcc\n" \
"v_accvgpr_write_b32 a85, v52\n" \
"s_add_u32 m0, s65, 25600\n" \
"s_mov_b32 s59, 9984\n" \
"buffer_load_dwordx4 v11, s[40:43], s59 offen lds\n" \
"v_cmp_eq_u32 vcc, -6, v50\n" \
"s_nop 1\n" \
"v_cndmask_b32 v52, 0, v51, vcc\n" \
"v_accvgpr_write_b32 a86, v52\n" \
"v_cmp_eq_u32 vcc, -5, v50\n" \
"s_add_u32 m0, s65, 26624\n" \
"s_mov_b32 s59, 19200\n" \
"buffer_load_dwordx4 v1, s[40:43], s59 offen lds\n" \
"s_nop 1\n" \
"v_cndmask_b32 v52, 0, v51, vcc\n" \
"v_accvgpr_write_b32 a87, v52\n" \
"v_mov_b32 v52, 0x3f803f80\n" \
"v_accvgpr_write_b32 a88, v52\n" \
"s_add_u32 m0, s65, 27648\n" \
"s_mov_b32 s59, 28416\n" \
"buffer_load_dwordx4 v11, s[40:43], s59 offen lds\n" \
"v_accvgpr_write_b32 a89, v52\n" \
"v_accvgpr_write_b32 a90, v52\n" \
"v_accvgpr_write_b32 a91, v52\n" \
"v_lshrrev_b32 v48, 2, v0\n" \
"v_and_b32 v48, 3, v48\n" \
"s_add_u32 m0, s65, 28672\n" \
"s_mov_b32 s59, 896\n" \
"buffer_load_dwordx4 v1, s[40:43], s59 offen lds\n" \
"v_lshlrev_b32 v6, 7, v48\n" \
"v_lshrrev_b32 v48, 1, v48\n" \
"v_lshl_add_u32 v6, v48, 5, v6\n" \
"v_lshrrev_b32 v48, 4, v0\n" \
"v_lshl_add_u32 v6, v48, 10, v6\n" \
"s_add_u32 m0, s65, 29696\n" \
"s_mov_b32 s59, 10112\n" \
"buffer_load_dwordx4 v11, s[40:43], s59 offen lds\n" \
"v_and_b32 v49, 1, v48\n" \
"v_lshl_add_u32 v6, v49, 6, v6\n" \
"v_and_b32 v49, 3, v0\n" \
"v_lshl_add_u32 v6, v49, 3, v6\n" \
"v_add_u32 v98, 147456, v6\n" \
"s_add_u32 m0, s65, 30720\n" \
"s_mov_b32 s59, 19328\n" \
"buffer_load_dwordx4 v1, s[40:43], s59 offen lds\n" \
"v_xor_b32 v99, 32, v98\n" \
"v_xor_b32 v100, 64, v98\n" \
"v_xor_b32 v101, 96, v98\n" \
"v_mov_b32 v102, v6\n" \
"v_xor_b32 v103, 32, v6\n" \
"s_add_u32 m0, s65, 31744\n" \
"s_mov_b32 s59, 28544\n" \
"buffer_load_dwordx4 v11, s[40:43], s59 offen lds\n" \
"v_xor_b32 v104, 64, v6\n" \
"v_xor_b32 v105, 96, v6\n" \
"v_add_u32 v106, 24576, v6\n" \
"v_xor_b32 v107, 32, v106\n" \
"v_xor_b32 v108, 64, v106\n" \
"s_add_u32 m0, s65, 32768\n" \
"s_mov_b32 s59, 1024\n" \
"buffer_load_dwordx4 v1, s[40:43], s59 offen lds\n" \
"v_xor_b32 v109, 96, v106\n" \
"v_add_u32 v7, 73728, v6\n" \
"v_lshlrev_b32 v8, 4, v0\n" \
"v_and_b32 v13, 15, v0\n" \
"v_lshlrev_b32 v9, 7, v13\n" \
"s_add_u32 m0, s65, 33792\n" \
"s_mov_b32 s59, 10240\n" \
"buffer_load_dwordx4 v11, s[40:43], s59 offen lds\n" \
"v_add_u32 v9, s65, v9\n" \
"v_lshrrev_b32 v13, 1, v13\n" \
"v_xor_b32 v13, v13, v48\n" \
"v_lshl_add_u32 v96, v13, 4, v9\n" \
"v_xor_b32 v97, 64, v96\n" \
"s_add_u32 m0, s65, 34816\n" \
"s_mov_b32 s59, 19456\n" \
"buffer_load_dwordx4 v1, s[40:43], s59 offen lds\n" \
"s_add_u32 m0, s65, 35840\n" \
"s_mov_b32 s59, 28672\n" \
"buffer_load_dwordx4 v11, s[40:43], s59 offen lds\n" \
"s_add_u32 s59, s56, 0\n" \
"s_add_u32 m0, s58, 147456\n" \
"s_nop 0\n" \
"buffer_load_dwordx4 v2, s[44:47], s59 offen lds\n" \
"s_add_u32 s59, s56, 294912\n" \
"s_add_u32 m0, s58, 151552\n" \
"s_nop 0\n" \
"buffer_load_dwordx4 v2, s[44:47], s59 offen lds\n" \
"s_add_u32 s59, s56, 589824\n" \
"s_add_u32 m0, s58, 155648\n" \
"s_nop 0\n" \
"buffer_load_dwordx4 v2, s[44:47], s59 offen lds\n" \
"s_add_u32 s59, s56, 884736\n" \
"s_add_u32 m0, s58, 159744\n" \
"s_nop 0\n" \
"buffer_load_dwordx4 v2, s[44:47], s59 offen lds\n" \
"v_mov_b32 v48, 0\n" \
"global_load_dword v13, v48, s[84:85] sc1\n" \
"v_add_u32 v4, 49152, v6\n" \
"v_xor_b32 v5, 32, v4\n" \
"v_xor_b32 v11, 64, v4\n" \
"v_xor_b32 v0, 96, v4\n" \
"s_waitcnt vmcnt(37)\n" \
"ds_read_b128 v[160:163], v96 offset:0\n" \
"ds_read_b128 v[164:167], v96 offset:2048\n" \
"ds_read_b128 v[168:171], v97 offset:0\n" \
"ds_read_b128 v[172:175], v97 offset:2048\n" \
"s_waitcnt vmcnt(33)\n" \
"ds_read_b128 v[176:179], v96 offset:4096\n" \
"ds_read_b128 v[180:183], v96 offset:6144\n" \
"ds_read_b128 v[184:187], v97 offset:4096\n" \
"ds_read_b128 v[188:191], v97 offset:6144\n" \
"s_waitcnt lgkmcnt(4)\n" \
"v_mfma_f32_16x16x32_bf16 v[64:67], a[88:91], v[160:163], 0\n" \
"v_mfma_f32_16x16x32_bf16 v[68:71], a[88:91], v[164:167], 0\n" \
"v_mfma_f32_16x16x32_bf16 v[64:67], a[88:91], v[168:171], v[64:67]\n" \
"v_mfma_f32_16x16x32_bf16 v[68:71], a[88:91], v[172:175], v[68:71]\n" \
"s_waitcnt vmcnt(29)\n" \
"ds_read_b128 v[192:195], v96 offset:8192\n" \
"ds_read_b128 v[196:199], v96 offset:10240\n" \
"ds_read_b128 v[200:203], v97 offset:8192\n" \
"ds_read_b128 v[204:207], v97 offset:10240\n" \
"s_waitcnt lgkmcnt(4)\n" \
"v_mfma_f32_16x16x32_bf16 v[64:67], a[88:91], v[176:179], v[64:67]\n" \
"v_mfma_f32_16x16x32_bf16 v[68:71], a[88:91], v[180:183], v[68:71]\n" \
"v_mfma_f32_16x16x32_bf16 v[64:67], a[88:91], v[184:187], v[64:67]\n" \
"v_mfma_f32_16x16x32_bf16 v[68:71], a[88:91], v[188:191], v[68:71]\n" \
"s_waitcnt vmcnt(25)\n" \
"ds_read_b128 v[208:211], v96 offset:12288\n" \
"ds_read_b128 v[212:215], v96 offset:14336\n" \
"ds_read_b128 v[216:219], v97 offset:12288\n" \
"ds_read_b128 v[220:223], v97 offset:14336\n" \
"s_waitcnt lgkmcnt(4)\n" \
"v_mfma_f32_16x16x32_bf16 v[64:67], a[88:91], v[192:195], v[64:67]\n" \
"v_mfma_f32_16x16x32_bf16 v[68:71], a[88:91], v[196:199], v[68:71]\n" \
"v_mfma_f32_16x16x32_bf16 v[64:67], a[88:91], v[200:203], v[64:67]\n" \
"v_mfma_f32_16x16x32_bf16 v[68:71], a[88:91], v[204:207], v[68:71]\n" \
"s_waitcnt vmcnt(21)\n" \
"ds_read_b128 v[224:227], v96 offset:16384\n" \
"ds_read_b128 v[228:231], v96 offset:18432\n" \
"ds_read_b128 v[232:235], v97 offset:16384\n" \
"ds_read_b128 v[236:239], v97 offset:18432\n" \
"s_waitcnt lgkmcnt(4)\n" \
"v_mfma_f32_16x16x32_bf16 v[64:67], a[88:91], v[208:211], v[64:67]\n" \
"v_mfma_f32_16x16x32_bf16 v[68:71], a[88:91], v[212:215], v[68:71]\n" \
"v_mfma_f32_16x16x32_bf16 v[64:67], a[88:91], v[216:219], v[64:67]\n" \
"v_mfma_f32_16x16x32_bf16 v[68:71], a[88:91], v[220:223], v[68:71]\n" \
"s_waitcnt vmcnt(17)\n" \
"ds_read_b128 v[240:243], v96 offset:20480\n" \
"ds_read_b128 v[244:247], v96 offset:22528\n" \
"ds_read_b128 v[248:251], v97 offset:20480\n" \
"ds_read_b128 v[252:255], v97 offset:22528\n" \
"s_waitcnt lgkmcnt(4)\n" \
"v_mfma_f32_16x16x32_bf16 v[64:67], a[88:91], v[224:227], v[64:67]\n" \
"v_mfma_f32_16x16x32_bf16 v[68:71], a[88:91], v[228:231], v[68:71]\n" \
"v_mfma_f32_16x16x32_bf16 v[64:67], a[88:91], v[232:235], v[64:67]\n" \
"v_mfma_f32_16x16x32_bf16 v[68:71], a[88:91], v[236:239], v[68:71]\n" \
"s_waitcnt vmcnt(13)\n" \
"ds_read_b128 v[112:115], v96 offset:24576\n" \
"ds_read_b128 v[116:119], v96 offset:26624\n" \
"ds_read_b128 v[120:123], v97 offset:24576\n" \
"ds_read_b128 v[124:127], v97 offset:26624\n" \
"s_waitcnt lgkmcnt(4)\n" \
"v_mfma_f32_16x16x32_bf16 v[64:67], a[88:91], v[240:243], v[64:67]\n" \
"v_mfma_f32_16x16x32_bf16 v[68:71], a[88:91], v[244:247], v[68:71]\n" \
"v_mfma_f32_16x16x32_bf16 v[64:67], a[88:91], v[248:251], v[64:67]\n" \
"v_mfma_f32_16x16x32_bf16 v[68:71], a[88:91], v[252:255], v[68:71]\n" \
"s_waitcnt vmcnt(9)\n" \
"ds_read_b128 v[128:131], v96 offset:28672\n" \
"ds_read_b128 v[132:135], v96 offset:30720\n" \
"ds_read_b128 v[136:139], v97 offset:28672\n" \
"ds_read_b128 v[140:143], v97 offset:30720\n" \
"s_waitcnt lgkmcnt(4)\n" \
"v_mfma_f32_16x16x32_bf16 v[64:67], a[88:91], v[112:115], v[64:67]\n" \
"v_mfma_f32_16x16x32_bf16 v[68:71], a[88:91], v[116:119], v[68:71]\n" \
"v_mfma_f32_16x16x32_bf16 v[64:67], a[88:91], v[120:123], v[64:67]\n" \
"v_mfma_f32_16x16x32_bf16 v[68:71], a[88:91], v[124:127], v[68:71]\n" \
"s_waitcnt vmcnt(5)\n" \
"ds_read_b128 v[144:147], v96 offset:32768\n" \
"ds_read_b128 v[148:151], v96 offset:34816\n" \
"ds_read_b128 v[152:155], v97 offset:32768\n" \
"ds_read_b128 v[156:159], v97 offset:34816\n" \
"s_waitcnt lgkmcnt(4)\n" \
"v_mfma_f32_16x16x32_bf16 v[64:67], a[88:91], v[128:131], v[64:67]\n" \
"v_mfma_f32_16x16x32_bf16 v[68:71], a[88:91], v[132:135], v[68:71]\n" \
"v_mfma_f32_16x16x32_bf16 v[64:67], a[88:91], v[136:139], v[64:67]\n" \
"v_mfma_f32_16x16x32_bf16 v[68:71], a[88:91], v[140:143], v[68:71]\n" \
"s_waitcnt vmcnt(0)\n" \
"s_waitcnt lgkmcnt(0)\n" \
"v_mfma_f32_16x16x32_bf16 v[64:67], a[88:91], v[144:147], v[64:67]\n" \
"v_mfma_f32_16x16x32_bf16 v[68:71], a[88:91], v[148:151], v[68:71]\n" \
"v_mfma_f32_16x16x32_bf16 v[64:67], a[88:91], v[152:155], v[64:67]\n" \
"v_mfma_f32_16x16x32_bf16 v[68:71], a[88:91], v[156:159], v[68:71]\n" \
"s_barrier\n" \
"v_add_u32 v13, 1, v13\n" \
"v_mov_b32 v48, s87\n" \
"v_mov_b32 v1, s86\n" \
"v_readfirstlane_b32 s88, v13\n" \
"s_cmp_eq_u32 s61, 0\n" \
"s_cselect_b32 s89, 2, 99\n" \
"s_cbranch_scc0 Lack_%=\n" \
"global_store_dword v48, v13, s[84:85] sc1\n" \
"Lack_%=:\n" \
"s_add_u32 s59, s56, 1179648\n" \
"s_add_u32 m0, s58, 16384\n" \
"s_nop 0\n" \
"buffer_load_dwordx4 v2, s[44:47], s59 offen lds\n" \
"s_add_u32 s59, s56, 1474560\n" \
"s_add_u32 m0, s58, 20480\n" \
"s_nop 0\n" \
"buffer_load_dwordx4 v2, s[44:47], s59 offen lds\n" \
"v_mul_f32 v64, 0xbae38e39, v64\n" \
"v_mul_f32 v68, 0xbae38e39, v68\n" \
"v_cvt_pk_bf16_f32 v64, v64, v64\n" \
"v_cvt_pk_bf16_f32 v68, v68, v68\n" \
"v_and_b32 v65, 0xffff0000, v64\n" \
"v_and_b32 v66, 0xffff0000, v64\n" \
"v_and_b32 v67, 0xffff0000, v64\n" \
"v_and_b32 v64, 0xffff0000, v64\n" \
"v_and_b32 v69, 0xffff0000, v68\n" \
"v_and_b32 v70, 0xffff0000, v68\n" \
"v_and_b32 v71, 0xffff0000, v68\n" \
"v_and_b32 v68, 0xffff0000, v68\n" \
"s_add_u32 s59, s56, 1769472\n" \
"s_add_u32 m0, s58, 24576\n" \
"s_nop 0\n" \
"buffer_load_dwordx4 v2, s[44:47], s59 offen lds\n" \
"ds_read_b64_tr_b16 a[48:49], v98 offset:0\n" \
"ds_read_b64_tr_b16 a[50:51], v98 offset:512\n" \
"ds_read_b64_tr_b16 a[52:53], v99 offset:0\n" \
"ds_read_b64_tr_b16 a[54:55], v99 offset:512\n" \
"ds_read_b64_tr_b16 a[56:57], v100 offset:0\n" \
"ds_read_b64_tr_b16 a[58:59], v100 offset:512\n" \
"ds_read_b64_tr_b16 a[60:61], v101 offset:0\n" \
"ds_read_b64_tr_b16 a[62:63], v101 offset:512\n" \
"ds_read_b64_tr_b16 a[64:65], v98 offset:4096\n" \
"ds_read_b64_tr_b16 a[66:67], v98 offset:4608\n" \
"ds_read_b64_tr_b16 a[68:69], v99 offset:4096\n" \
"ds_read_b64_tr_b16 a[70:71], v99 offset:4608\n" \
"ds_read_b64_tr_b16 a[72:73], v100 offset:4096\n" \
"ds_read_b64_tr_b16 a[74:75], v100 offset:4608\n" \
"v_mfma_f32_16x16x32_bf16 v[72:75], a[80:83], v[160:163], v[64:67]\n" \
"v_mfma_f32_16x16x32_bf16 v[76:79], a[84:87], v[160:163], v[64:67]\n" \
"v_mfma_f32_16x16x32_bf16 v[80:83], a[80:83], v[164:167], v[68:71]\n" \
"v_mfma_f32_16x16x32_bf16 v[84:87], a[84:87], v[164:167], v[68:71]\n" \
"s_nop 4\n" \
"s_add_u32 s59, s56, 2064384\n" \
"s_add_u32 m0, s58, 28672\n" \
"s_nop 0\n" \
"buffer_load_dwordx4 v2, s[44:47], s59 offen lds\n" \
"v_mfma_f32_16x16x32_bf16 v[88:91], a[80:83], v[168:171], v[64:67]\n" \
"v_mfma_f32_16x16x32_bf16 v[92:95], a[84:87], v[168:171], v[64:67]\n" \
"v_cvt_pk_bf16_f32 v160, v72, v73\n" \
"v_cvt_pk_bf16_f32 v161, v74, v75\n" \
"v_cvt_pk_bf16_f32 v162, v76, v77\n" \
"s_add_u32 s59, s56, 2359296\n" \
"s_add_u32 m0, s58, 32768\n" \
"s_nop 0\n" \
"buffer_load_dwordx4 v2, s[44:47], s59 offen lds\n" \
"v_cvt_pk_bf16_f32 v163, v78, v79\n" \
"v_mfma_f32_16x16x32_bf16 v[72:75], a[80:83], v[172:175], v[68:71]\n" \
"v_mfma_f32_16x16x32_bf16 v[76:79], a[84:87], v[172:175], v[68:71]\n" \
"v_cvt_pk_bf16_f32 v164, v80, v81\n" \
"v_cvt_pk_bf16_f32 v165, v82, v83\n" \
"s_add_u32 s59, s56, 2654208\n" \
"s_add_u32 m0, s58, 36864\n" \
"s_nop 0\n" \
"buffer_load_dwordx4 v2, s[44:47], s59 offen lds\n" \
"v_cvt_pk_bf16_f32 v166, v84, v85\n" \
"v_cvt_pk_bf16_f32 v167, v86, v87\n" \
"v_mfma_f32_16x16x32_bf16 v[80:83], a[80:83], v[176:179], v[64:67]\n" \
"v_mfma_f32_16x16x32_bf16 v[84:87], a[84:87], v[176:179], v[64:67]\n" \
"v_cvt_pk_bf16_f32 v168, v88, v89\n" \
"s_add_u32 s59, s56, 2949120\n" \
"s_add_u32 m0, s58, 40960\n" \
"s_nop 0\n" \
"buffer_load_dwordx4 v2, s[44:47], s59 offen lds\n" \
"v_cvt_pk_bf16_f32 v169, v90, v91\n" \
"v_cvt_pk_bf16_f32 v170, v92, v93\n" \
"v_cvt_pk_bf16_f32 v171, v94, v95\n" \
"v_mfma_f32_16x16x32_bf16 v[88:91], a[80:83], v[180:183], v[68:71]\n" \
"v_mfma_f32_16x16x32_bf16 v[92:95], a[84:87], v[180:183], v[68:71]\n" \
"s_add_u32 s59, s56, 3244032\n" \
"s_add_u32 m0, s58, 45056\n" \
"s_nop 0\n" \
"buffer_load_dwordx4 v2, s[44:47], s59 offen lds\n" \
"v_cvt_pk_bf16_f32 v172, v72, v73\n" \
"v_cvt_pk_bf16_f32 v173, v74, v75\n" \
"v_cvt_pk_bf16_f32 v174, v76, v77\n" \
"v_cvt_pk_bf16_f32 v175, v78, v79\n" \
"s_waitcnt lgkmcnt(6)\n" \
"v_mfma_f32_16x16x32_bf16 v[16:19], a[48:51], v[160:163], 0\n" \
"ds_read_b64_tr_b16 a[76:77], v101 offset:4096\n" \
"ds_read_b64_tr_b16 a[78:79], v101 offset:4608\n" \
"s_add_u32 s59, s56, 3538944\n" \
"s_add_u32 m0, s58, 49152\n" \
"v_mfma_f32_16x16x32_bf16 v[20:23], a[48:51], v[164:167], 0\n" \
"v_mfma_f32_16x16x32_bf16 v[72:75], a[80:83], v[184:187], v[64:67]\n" \
"v_mfma_f32_16x16x32_bf16 v[76:79], a[84:87], v[184:187], v[64:67]\n" \
"v_cvt_pk_bf16_f32 v176, v80, v81\n" \
"v_cvt_pk_bf16_f32 v177, v82, v83\n" \
"v_mfma_f32_16x16x32_bf16 v[24:27], a[52:55], v[160:163], 0\n" \
"ds_read_b64_tr_b16 a[48:49], v98 offset:8192\n" \
"ds_read_b64_tr_b16 a[50:51], v98 offset:8704\n" \
"buffer_load_dwordx4 v2, s[44:47], s59 offen lds\n" \
"v_mfma_f32_16x16x32_bf16 v[28:31], a[52:55], v[164:167], 0\n" \
"v_cvt_pk_bf16_f32 v178, v84, v85\n" \
"v_cvt_pk_bf16_f32 v179, v86, v87\n" \
"v_mfma_f32_16x16x32_bf16 v[80:83], a[80:83], v[188:191], v[68:71]\n" \
"v_mfma_f32_16x16x32_bf16 v[84:87], a[84:87], v[188:191], v[68:71]\n" \
"v_mfma_f32_16x16x32_bf16 v[32:35], a[56:59], v[160:163], 0\n" \
"ds_read_b64_tr_b16 a[52:53], v99 offset:8192\n" \
"ds_read_b64_tr_b16 a[54:55], v99 offset:8704\n" \
"s_add_u32 s59, s56, 3833856\n" \
"s_add_u32 m0, s58, 53248\n" \
"v_mfma_f32_16x16x32_bf16 v[36:39], a[56:59], v[164:167], 0\n" \
"v_cvt_pk_bf16_f32 v180, v88, v89\n" \
"v_cvt_pk_bf16_f32 v181, v90, v91\n" \
"v_cvt_pk_bf16_f32 v182, v92, v93\n" \
"v_cvt_pk_bf16_f32 v183, v94, v95\n" \
"v_mfma_f32_16x16x32_bf16 v[88:91], a[80:83], v[192:195], v[64:67]\n" \
"v_mfma_f32_16x16x32_bf16 v[40:43], a[60:63], v[160:163], 0\n" \
"ds_read_b64_tr_b16 a[56:57], v100 offset:8192\n" \
"ds_read_b64_tr_b16 a[58:59], v100 offset:8704\n" \
"buffer_load_dwordx4 v2, s[44:47], s59 offen lds\n" \
"v_mfma_f32_16x16x32_bf16 v[92:95], a[84:87], v[192:195], v[64:67]\n" \
"v_mfma_f32_16x16x32_bf16 v[44:47], a[60:63], v[164:167], 0\n" \
"v_cvt_pk_bf16_f32 v184, v72, v73\n" \
"v_cvt_pk_bf16_f32 v185, v74, v75\n" \
"v_cvt_pk_bf16_f32 v186, v76, v77\n" \
"v_cvt_pk_bf16_f32 v187, v78, v79\n" \
"s_waitcnt lgkmcnt(6)\n" \
"v_mfma_f32_16x16x32_bf16 v[16:19], a[64:67], v[168:171], v[16:19]\n" \
"ds_read_b64_tr_b16 a[60:61], v101 offset:8192\n" \
"ds_read_b64_tr_b16 a[62:63], v101 offset:8704\n" \
"s_add_u32 s59, s56, 4128768\n" \
"s_add_u32 m0, s58, 57344\n" \
"v_mfma_f32_16x16x32_bf16 v[20:23], a[64:67], v[172:175], v[20:23]\n" \
"v_mfma_f32_16x16x32_bf16 v[72:75], a[80:83], v[196:199], v[68:71]\n" \
"v_mfma_f32_16x16x32_bf16 v[76:79], a[84:87], v[196:199], v[68:71]\n" \
"v_cvt_pk_bf16_f32 v188, v80, v81\n" \
"v_cvt_pk_bf16_f32 v189, v82, v83\n" \
"v_mfma_f32_16x16x32_bf16 v[24:27], a[68:71], v[168:171], v[24:27]\n" \
"ds_read_b64_tr_b16 a[64:65], v98 offset:12288\n" \
"ds_read_b64_tr_b16 a[66:67], v98 offset:12800\n" \
"buffer_load_dwordx4 v2, s[44:47], s59 offen lds\n" \
"v_mfma_f32_16x16x32_bf16 v[28:31], a[68:71], v[172:175], v[28:31]\n" \
"v_cvt_pk_bf16_f32 v190, v84, v85\n" \
"v_cvt_pk_bf16_f32 v191, v86, v87\n" \
"v_mfma_f32_16x16x32_bf16 v[80:83], a[80:83], v[200:203], v[64:67]\n" \
"v_mfma_f32_16x16x32_bf16 v[84:87], a[84:87], v[200:203], v[64:67]\n" \
"v_mfma_f32_16x16x32_bf16 v[32:35], a[72:75], v[168:171], v[32:35]\n" \
"ds_read_b64_tr_b16 a[68:69], v99 offset:12288\n" \
"ds_read_b64_tr_b16 a[70:71], v99 offset:12800\n" \
"s_add_u32 s59, s56, 4423680\n" \
"s_add_u32 m0, s58, 61440\n" \
"v_mfma_f32_16x16x32_bf16 v[36:39], a[72:75], v[172:175], v[36:39]\n" \
"v_cvt_pk_bf16_f32 v192, v88, v89\n" \
"v_cvt_pk_bf16_f32 v193, v90, v91\n" \
"v_cvt_pk_bf16_f32 v194, v92, v93\n" \
"v_cvt_pk_bf16_f32 v195, v94, v95\n" \
"v_mfma_f32_16x16x32_bf16 v[88:91], a[80:83], v[204:207], v[68:71]\n" \
"v_mfma_f32_16x16x32_bf16 v[40:43], a[76:79], v[168:171], v[40:43]\n" \
"ds_read_b64_tr_b16 a[72:73], v100 offset:12288\n" \
"ds_read_b64_tr_b16 a[74:75], v100 offset:12800\n" \
"buffer_load_dwordx4 v2, s[44:47], s59 offen lds\n" \
"v_mfma_f32_16x16x32_bf16 v[92:95], a[84:87], v[204:207], v[68:71]\n" \
"v_mfma_f32_16x16x32_bf16 v[44:47], a[76:79], v[172:175], v[44:47]\n" \
"v_cvt_pk_bf16_f32 v196, v72, v73\n" \
"v_cvt_pk_bf16_f32 v197, v74, v75\n" \
"v_cvt_pk_bf16_f32 v198, v76, v77\n" \
"v_cvt_pk_bf16_f32 v199, v78, v79\n" \
"s_waitcnt lgkmcnt(6)\n" \
"v_mfma_f32_16x16x32_bf16 v[16:19], a[48:51], v[176:179], v[16:19]\n" \
"ds_read_b64_tr_b16 a[76:77], v101 offset:12288\n" \
"ds_read_b64_tr_b16 a[78:79], v101 offset:12800\n" \
"s_waitcnt vmcnt(10)\n" \
"s_barrier\n" \
"s_add_u32 s59, s56, 4718592\n" \
"v_mfma_f32_16x16x32_bf16 v[20:23], a[48:51], v[180:183], v[20:23]\n" \
"s_add_u32 m0, s58, 65536\n" \
"v_mfma_f32_16x16x32_bf16 v[72:75], a[80:83], v[208:211], v[64:67]\n" \
"v_mfma_f32_16x16x32_bf16 v[76:79], a[84:87], v[208:211], v[64:67]\n" \
"v_cvt_pk_bf16_f32 v200, v80, v81\n" \
"v_cvt_pk_bf16_f32 v201, v82, v83\n" \
"v_mfma_f32_16x16x32_bf16 v[24:27], a[52:55], v[176:179], v[24:27]\n" \
"ds_read_b64_tr_b16 a[48:49], v102 offset:16384\n" \
"ds_read_b64_tr_b16 a[50:51], v102 offset:16896\n" \
"buffer_load_dwordx4 v2, s[44:47], s59 offen lds\n" \
"v_mfma_f32_16x16x32_bf16 v[28:31], a[52:55], v[180:183], v[28:31]\n" \
"v_cvt_pk_bf16_f32 v202, v84, v85\n" \
"v_cvt_pk_bf16_f32 v203, v86, v87\n" \
"v_mfma_f32_16x16x32_bf16 v[80:83], a[80:83], v[212:215], v[68:71]\n" \
"v_mfma_f32_16x16x32_bf16 v[84:87], a[84:87], v[212:215], v[68:71]\n" \
"v_mfma_f32_16x16x32_bf16 v[32:35], a[56:59], v[176:179], v[32:35]\n" \
"ds_read_b64_tr_b16 a[52:53], v103 offset:16384\n" \
"ds_read_b64_tr_b16 a[54:55], v103 offset:16896\n" \
"s_add_u32 s59, s56, 5013504\n" \
"s_add_u32 m0, s58, 69632\n" \
"v_mfma_f32_16x16x32_bf16 v[36:39], a[56:59], v[180:183], v[36:39]\n" \
"v_cvt_pk_bf16_f32 v204, v88, v89\n" \
"v_cvt_pk_bf16_f32 v205, v90, v91\n" \
"v_cvt_pk_bf16_f32 v206, v92, v93\n" \
"v_cvt_pk_bf16_f32 v207, v94, v95\n" \
"v_mfma_f32_16x16x32_bf16 v[88:91], a[80:83], v[216:219], v[64:67]\n" \
"v_mfma_f32_16x16x32_bf16 v[40:43], a[60:63], v[176:179], v[40:43]\n" \
"ds_read_b64_tr_b16 a[56:57], v104 offset:16384\n" \
"ds_read_b64_tr_b16 a[58:59], v104 offset:16896\n" \
"buffer_load_dwordx4 v2, s[44:47], s59 offen lds\n" \
"v_mfma_f32_16x16x32_bf16 v[92:95], a[84:87], v[216:219], v[64:67]\n" \
"v_mfma_f32_16x16x32_bf16 v[44:47], a[60:63], v[180:183], v[44:47]\n" \
"v_cvt_pk_bf16_f32 v208, v72, v73\n" \
"v_cvt_pk_bf16_f32 v209, v74, v75\n" \
"v_cvt_pk_bf16_f32 v210, v76, v77\n" \
"v_cvt_pk_bf16_f32 v211, v78, v79\n" \
"s_waitcnt lgkmcnt(6)\n" \
"v_mfma_f32_16x16x32_bf16 v[16:19], a[64:67], v[184:187], v[16:19]\n" \
"ds_read_b64_tr_b16 a[60:61], v105 offset:16384\n" \
"ds_read_b64_tr_b16 a[62:63], v105 offset:16896\n" \
"s_add_u32 s59, s56, 128\n" \
"s_add_u32 m0, s58, 73728\n" \
"v_mfma_f32_16x16x32_bf16 v[20:23], a[64:67], v[188:191], v[20:23]\n" \
"v_mfma_f32_16x16x32_bf16 v[72:75], a[80:83], v[220:223], v[68:71]\n" \
"v_mfma_f32_16x16x32_bf16 v[76:79], a[84:87], v[220:223], v[68:71]\n" \
"v_cvt_pk_bf16_f32 v212, v80, v81\n" \
"v_cvt_pk_bf16_f32 v213, v82, v83\n" \
"v_mfma_f32_16x16x32_bf16 v[24:27], a[68:71], v[184:187], v[24:27]\n" \
"ds_read_b64_tr_b16 a[64:65], v102 offset:20480\n" \
"ds_read_b64_tr_b16 a[66:67], v102 offset:20992\n" \
"buffer_load_dwordx4 v2, s[44:47], s59 offen lds\n" \
"v_mfma_f32_16x16x32_bf16 v[28:31], a[68:71], v[188:191], v[28:31]\n" \
"v_cvt_pk_bf16_f32 v214, v84, v85\n" \
"v_cvt_pk_bf16_f32 v215, v86, v87\n" \
"v_mfma_f32_16x16x32_bf16 v[80:83], a[80:83], v[224:227], v[64:67]\n" \
"v_mfma_f32_16x16x32_bf16 v[84:87], a[84:87], v[224:227], v[64:67]\n" \
"v_mfma_f32_16x16x32_bf16 v[32:35], a[72:75], v[184:187], v[32:35]\n" \
"ds_read_b64_tr_b16 a[68:69], v103 offset:20480\n" \
"ds_read_b64_tr_b16 a[70:71], v103 offset:20992\n" \
"s_add_u32 s59, s56, 295040\n" \
"s_add_u32 m0, s58, 77824\n" \
"v_mfma_f32_16x16x32_bf16 v[36:39], a[72:75], v[188:191], v[36:39]\n" \
"v_cvt_pk_bf16_f32 v216, v88, v89\n" \
"v_cvt_pk_bf16_f32 v217, v90, v91\n" \
"v_cvt_pk_bf16_f32 v218, v92, v93\n" \
"v_cvt_pk_bf16_f32 v219, v94, v95\n" \
"v_mfma_f32_16x16x32_bf16 v[88:91], a[80:83], v[228:231], v[68:71]\n" \
"v_mfma_f32_16x16x32_bf16 v[40:43], a[76:79], v[184:187], v[40:43]\n" \
"ds_read_b64_tr_b16 a[72:73], v104 offset:20480\n" \
"ds_read_b64_tr_b16 a[74:75], v104 offset:20992\n" \
"buffer_load_dwordx4 v2, s[44:47], s59 offen lds\n" \
"v_mfma_f32_16x16x32_bf16 v[92:95], a[84:87], v[228:231], v[68:71]\n" \
"v_mfma_f32_16x16x32_bf16 v[44:47], a[76:79], v[188:191], v[44:47]\n" \
"v_cvt_pk_bf16_f32 v220, v72, v73\n" \
"v_cvt_pk_bf16_f32 v221, v74, v75\n" \
"v_cvt_pk_bf16_f32 v222, v76, v77\n" \
"v_cvt_pk_bf16_f32 v223, v78, v79\n" \
"s_waitcnt lgkmcnt(6)\n" \
"v_mfma_f32_16x16x32_bf16 v[16:19], a[48:51], v[192:195], v[16:19]\n" \
"ds_read_b64_tr_b16 a[76:77], v105 offset:20480\n" \
"ds_read_b64_tr_b16 a[78:79], v105 offset:20992\n" \
"s_waitcnt vmcnt(8)\n" \
"s_barrier\n" \
"s_add_u32 s59, s56, 589952\n" \
"v_mfma_f32_16x16x32_bf16 v[20:23], a[48:51], v[196:199], v[20:23]\n" \
"s_add_u32 m0, s58, 81920\n" \
"v_mfma_f32_16x16x32_bf16 v[72:75], a[80:83], v[232:235], v[64:67]\n" \
"v_mfma_f32_16x16x32_bf16 v[76:79], a[84:87], v[232:235], v[64:67]\n" \
"v_cvt_pk_bf16_f32 v224, v80, v81\n" \
"v_cvt_pk_bf16_f32 v225, v82, v83\n" \
"v_mfma_f32_16x16x32_bf16 v[24:27], a[52:55], v[192:195], v[24:27]\n" \
"ds_read_b64_tr_b16 a[48:49], v106 offset:0\n" \
"ds_read_b64_tr_b16 a[50:51], v106 offset:512\n" \
"buffer_load_dwordx4 v2, s[44:47], s59 offen lds\n" \
"v_mfma_f32_16x16x32_bf16 v[28:31], a[52:55], v[196:199], v[28:31]\n" \
"v_cvt_pk_bf16_f32 v226, v84, v85\n" \
"v_cvt_pk_bf16_f32 v227, v86, v87\n" \
"v_mfma_f32_16x16x32_bf16 v[80:83], a[80:83], v[236:239], v[68:71]\n" \
"v_mfma_f32_16x16x32_bf16 v[84:87], a[84:87], v[236:239], v[68:71]\n" \
"v_mfma_f32_16x16x32_bf16 v[32:35], a[56:59], v[192:195], v[32:35]\n" \
"ds_read_b64_tr_b16 a[52:53], v107 offset:0\n" \
"ds_read_b64_tr_b16 a[54:55], v107 offset:512\n" \
"s_add_u32 s59, s56, 884864\n" \
"s_add_u32 m0, s58, 86016\n" \
"v_mfma_f32_16x16x32_bf16 v[36:39], a[56:59], v[196:199], v[36:39]\n" \
"v_cvt_pk_bf16_f32 v228, v88, v89\n" \
"v_cvt_pk_bf16_f32 v229, v90, v91\n" \
"v_cvt_pk_bf16_f32 v230, v92, v93\n" \
"v_cvt_pk_bf16_f32 v231, v94, v95\n" \
"v_mfma_f32_16x16x32_bf16 v[88:91], a[80:83], v[240:243], v[64:67]\n" \
"v_mfma_f32_16x16x32_bf16 v[40:43], a[60:63], v[192:195], v[40:43]\n" \
"ds_read_b64_tr_b16 a[56:57], v108 offset:0\n" \
"ds_read_b64_tr_b16 a[58:59], v108 offset:512\n" \
"buffer_load_dwordx4 v2, s[44:47], s59 offen lds\n" \
"v_mfma_f32_16x16x32_bf16 v[92:95], a[84:87], v[240:243], v[64:67]\n" \
"v_mfma_f32_16x16x32_bf16 v[44:47], a[60:63], v[196:199], v[44:47]\n" \
"v_cvt_pk_bf16_f32 v232, v72, v73\n" \
"v_cvt_pk_bf16_f32 v233, v74, v75\n" \
"v_cvt_pk_bf16_f32 v234, v76, v77\n" \
"v_cvt_pk_bf16_f32 v235, v78, v79\n" \
"s_waitcnt lgkmcnt(6)\n" \
"v_mfma_f32_16x16x32_bf16 v[16:19], a[64:67], v[200:203], v[16:19]\n" \
"ds_read_b64_tr_b16 a[60:61], v109 offset:0\n" \
"ds_read_b64_tr_b16 a[62:63], v109 offset:512\n" \
"s_add_u32 s59, s56, 1179776\n" \
"s_add_u32 m0, s58, 90112\n" \
"v_mfma_f32_16x16x32_bf16 v[20:23], a[64:67], v[204:207], v[20:23]\n" \
"v_mfma_f32_16x16x32_bf16 v[72:75], a[80:83], v[244:247], v[68:71]\n" \
"v_mfma_f32_16x16x32_bf16 v[76:79], a[84:87], v[244:247], v[68:71]\n" \
"v_cvt_pk_bf16_f32 v236, v80, v81\n" \
"v_cvt_pk_bf16_f32 v237, v82, v83\n" \
"v_mfma_f32_16x16x32_bf16 v[24:27], a[68:71], v[200:203], v[24:27]\n" \
"ds_read_b64_tr_b16 a[64:65], v106 offset:4096\n" \
"ds_read_b64_tr_b16 a[66:67], v106 offset:4608\n" \
"buffer_load_dwordx4 v2, s[44:47], s59 offen lds\n" \
"v_mfma_f32_16x16x32_bf16 v[28:31], a[68:71], v[204:207], v[28:31]\n" \
"v_cvt_pk_bf16_f32 v238, v84, v85\n" \
"v_cvt_pk_bf16_f32 v239, v86, v87\n" \
"v_mfma_f32_16x16x32_bf16 v[80:83], a[80:83], v[248:251], v[64:67]\n" \
"v_mfma_f32_16x16x32_bf16 v[84:87], a[84:87], v[248:251], v[64:67]\n" \
"v_mfma_f32_16x16x32_bf16 v[32:35], a[72:75], v[200:203], v[32:35]\n" \
"ds_read_b64_tr_b16 a[68:69], v107 offset:4096\n" \
"ds_read_b64_tr_b16 a[70:71], v107 offset:4608\n" \
"s_add_u32 s59, s56, 1474688\n" \
"s_add_u32 m0, s58, 94208\n" \
"v_mfma_f32_16x16x32_bf16 v[36:39], a[72:75], v[204:207], v[36:39]\n" \
"v_cvt_pk_bf16_f32 v240, v88, v89\n" \
"v_cvt_pk_bf16_f32 v241, v90, v91\n" \
"v_cvt_pk_bf16_f32 v242, v92, v93\n" \
"v_cvt_pk_bf16_f32 v243, v94, v95\n" \
"v_mfma_f32_16x16x32_bf16 v[88:91], a[80:83], v[252:255], v[68:71]\n" \
"v_mfma_f32_16x16x32_bf16 v[40:43], a[76:79], v[200:203], v[40:43]\n" \
"ds_read_b64_tr_b16 a[72:73], v108 offset:4096\n" \
"ds_read_b64_tr_b16 a[74:75], v108 offset:4608\n" \
"buffer_load_dwordx4 v2, s[44:47], s59 offen lds\n" \
"v_mfma_f32_16x16x32_bf16 v[92:95], a[84:87], v[252:255], v[68:71]\n" \
"v_mfma_f32_16x16x32_bf16 v[44:47], a[76:79], v[204:207], v[44:47]\n" \
"v_cvt_pk_bf16_f32 v244, v72, v73\n" \
"v_cvt_pk_bf16_f32 v245, v74, v75\n" \
"v_cvt_pk_bf16_f32 v246, v76, v77\n" \
"v_cvt_pk_bf16_f32 v247, v78, v79\n" \
"s_waitcnt lgkmcnt(6)\n" \
"v_mfma_f32_16x16x32_bf16 v[16:19], a[48:51], v[208:211], v[16:19]\n" \
"ds_read_b64_tr_b16 a[76:77], v109 offset:4096\n" \
"ds_read_b64_tr_b16 a[78:79], v109 offset:4608\n" \
"s_add_u32 s59, s56, 1769600\n" \
"s_add_u32 m0, s58, 98304\n" \
"v_mfma_f32_16x16x32_bf16 v[20:23], a[48:51], v[212:215], v[20:23]\n" \
"v_mfma_f32_16x16x32_bf16 v[72:75], a[80:83], v[112:115], v[64:67]\n" \
"v_mfma_f32_16x16x32_bf16 v[76:79], a[84:87], v[112:115], v[64:67]\n" \
"v_cvt_pk_bf16_f32 v248, v80, v81\n" \
"v_cvt_pk_bf16_f32 v249, v82, v83\n" \
"v_mfma_f32_16x16x32_bf16 v[24:27], a[52:55], v[208:211], v[24:27]\n" \
"ds_read_b64_tr_b16 a[48:49], v106 offset:8192\n" \
"ds_read_b64_tr_b16 a[50:51], v106 offset:8704\n" \
"buffer_load_dwordx4 v2, s[44:47], s59 offen lds\n" \
"v_mfma_f32_16x16x32_bf16 v[28:31], a[52:55], v[212:215], v[28:31]\n" \
"v_cvt_pk_bf16_f32 v250, v84, v85\n" \
"v_cvt_pk_bf16_f32 v251, v86, v87\n" \
"v_mfma_f32_16x16x32_bf16 v[80:83], a[80:83], v[116:119], v[68:71]\n" \
"v_mfma_f32_16x16x32_bf16 v[84:87], a[84:87], v[116:119], v[68:71]\n" \
"v_mfma_f32_16x16x32_bf16 v[32:35], a[56:59], v[208:211], v[32:35]\n" \
"ds_read_b64_tr_b16 a[52:53], v107 offset:8192\n" \
"ds_read_b64_tr_b16 a[54:55], v107 offset:8704\n" \
"s_add_u32 s59, s56, 2064512\n" \
"s_add_u32 m0, s58, 102400\n" \
"v_mfma_f32_16x16x32_bf16 v[36:39], a[56:59], v[212:215], v[36:39]\n" \
"v_cvt_pk_bf16_f32 v252, v88, v89\n" \
"v_cvt_pk_bf16_f32 v253, v90, v91\n" \
"v_cvt_pk_bf16_f32 v254, v92, v93\n" \
"v_cvt_pk_bf16_f32 v255, v94, v95\n" \
"v_mfma_f32_16x16x32_bf16 v[88:91], a[80:83], v[120:123], v[64:67]\n" \
"v_mfma_f32_16x16x32_bf16 v[40:43], a[60:63], v[208:211], v[40:43]\n" \
"ds_read_b64_tr_b16 a[56:57], v108 offset:8192\n" \
"ds_read_b64_tr_b16 a[58:59], v108 offset:8704\n" \
"buffer_load_dwordx4 v2, s[44:47], s59 offen lds\n" \
"v_mfma_f32_16x16x32_bf16 v[92:95], a[84:87], v[120:123], v[64:67]\n" \
"v_mfma_f32_16x16x32_bf16 v[44:47], a[60:63], v[212:215], v[44:47]\n" \
"v_cvt_pk_bf16_f32 v112, v72, v73\n" \
"v_cvt_pk_bf16_f32 v113, v74, v75\n" \
"v_cvt_pk_bf16_f32 v114, v76, v77\n" \
"v_cvt_pk_bf16_f32 v115, v78, v79\n" \
"s_waitcnt lgkmcnt(6)\n" \
"v_mfma_f32_16x16x32_bf16 v[16:19], a[64:67], v[216:219], v[16:19]\n" \
"ds_read_b64_tr_b16 a[60:61], v109 offset:8192\n" \
"ds_read_b64_tr_b16 a[62:63], v109 offset:8704\n" \
"s_add_u32 s59, s56, 2359424\n" \
"s_add_u32 m0, s58, 106496\n" \
"v_mfma_f32_16x16x32_bf16 v[20:23], a[64:67], v[220:223], v[20:23]\n" \
"v_accvgpr_write_b32 a0, v112\n" \
"v_accvgpr_write_b32 a1, v113\n" \
"v_accvgpr_write_b32 a2, v114\n" \
"v_accvgpr_write_b32 a3, v115\n" \
"v_mfma_f32_16x16x32_bf16 v[72:75], a[80:83], v[124:127], v[68:71]\n" \
"v_mfma_f32_16x16x32_bf16 v[24:27], a[68:71], v[216:219], v[24:27]\n" \
"ds_read_b64_tr_b16 a[64:65], v106 offset:12288\n" \
"ds_read_b64_tr_b16 a[66:67], v106 offset:12800\n" \
"buffer_load_dwordx4 v2, s[44:47], s59 offen lds\n" \
"v_mfma_f32_16x16x32_bf16 v[76:79], a[84:87], v[124:127], v[68:71]\n" \
"v_mfma_f32_16x16x32_bf16 v[28:31], a[68:71], v[220:223], v[28:31]\n" \
"v_cvt_pk_bf16_f32 v116, v80, v81\n" \
"v_cvt_pk_bf16_f32 v117, v82, v83\n" \
"v_cvt_pk_bf16_f32 v118, v84, v85\n" \
"v_cvt_pk_bf16_f32 v119, v86, v87\n" \
"v_mfma_f32_16x16x32_bf16 v[32:35], a[72:75], v[216:219], v[32:35]\n" \
"ds_read_b64_tr_b16 a[68:69], v107 offset:12288\n" \
"ds_read_b64_tr_b16 a[70:71], v107 offset:12800\n" \
"s_add_u32 s59, s56, 2654336\n" \
"s_add_u32 m0, s58, 110592\n" \
"v_mfma_f32_16x16x32_bf16 v[36:39], a[72:75], v[220:223], v[36:39]\n" \
"v_accvgpr_write_b32 a4, v116\n" \
"v_accvgpr_write_b32 a5, v117\n" \
"v_accvgpr_write_b32 a6, v118\n" \
"v_accvgpr_write_b32 a7, v119\n" \
"v_mfma_f32_16x16x32_bf16 v[80:83], a[80:83], v[128:131], v[64:67]\n" \
"v_mfma_f32_16x16x32_bf16 v[40:43], a[76:79], v[216:219], v[40:43]\n" \
"ds_read_b64_tr_b16 a[72:73], v108 offset:12288\n" \
"ds_read_b64_tr_b16 a[74:75], v108 offset:12800\n" \
"buffer_load_dwordx4 v2, s[44:47], s59 offen lds\n" \
"v_mfma_f32_16x16x32_bf16 v[84:87], a[84:87], v[128:131], v[64:67]\n" \
"v_mfma_f32_16x16x32_bf16 v[44:47], a[76:79], v[220:223], v[44:47]\n" \
"v_cvt_pk_bf16_f32 v120, v88, v89\n" \
"v_cvt_pk_bf16_f32 v121, v90, v91\n" \
"v_cvt_pk_bf16_f32 v122, v92, v93\n" \
"v_cvt_pk_bf16_f32 v123, v94, v95\n" \
"s_waitcnt lgkmcnt(6)\n" \
"v_mfma_f32_16x16x32_bf16 v[16:19], a[48:51], v[224:227], v[16:19]\n" \
"ds_read_b64_tr_b16 a[76:77], v109 offset:12288\n" \
"ds_read_b64_tr_b16 a[78:79], v109 offset:12800\n" \
"s_add_u32 s59, s56, 2949248\n" \
"s_add_u32 m0, s58, 114688\n" \
"v_mfma_f32_16x16x32_bf16 v[20:23], a[48:51], v[228:231], v[20:23]\n" \
"v_accvgpr_write_b32 a8, v120\n" \
"v_accvgpr_write_b32 a9, v121\n" \
"v_accvgpr_write_b32 a10, v122\n" \
"v_accvgpr_write_b32 a11, v123\n" \
"v_mfma_f32_16x16x32_bf16 v[88:91], a[80:83], v[132:135], v[68:71]\n" \
"v_mfma_f32_16x16x32_bf16 v[24:27], a[52:55], v[224:227], v[24:27]\n" \
"ds_read_b64_tr_b16 a[48:49], v106 offset:16384\n" \
"ds_read_b64_tr_b16 a[50:51], v106 offset:16896\n" \
"buffer_load_dwordx4 v2, s[44:47], s59 offen lds\n" \
"v_mfma_f32_16x16x32_bf16 v[92:95], a[84:87], v[132:135], v[68:71]\n" \
"v_mfma_f32_16x16x32_bf16 v[28:31], a[52:55], v[228:231], v[28:31]\n" \
"v_cvt_pk_bf16_f32 v124, v72, v73\n" \
"v_cvt_pk_bf16_f32 v125, v74, v75\n" \
"v_cvt_pk_bf16_f32 v126, v76, v77\n" \
"v_cvt_pk_bf16_f32 v127, v78, v79\n" \
"v_mfma_f32_16x16x32_bf16 v[32:35], a[56:59], v[224:227], v[32:35]\n" \
"ds_read_b64_tr_b16 a[52:53], v107 offset:16384\n" \
"ds_read_b64_tr_b16 a[54:55], v107 offset:16896\n" \
"s_add_u32 s59, s56, 3244160\n" \
"s_add_u32 m0, s58, 118784\n" \
"v_mfma_f32_16x16x32_bf16 v[36:39], a[56:59], v[228:231], v[36:39]\n" \
"v_accvgpr_write_b32 a12, v124\n" \
"v_accvgpr_write_b32 a13, v125\n" \
"v_accvgpr_write_b32 a14, v126\n" \
"v_accvgpr_write_b32 a15, v127\n" \
"v_mfma_f32_16x16x32_bf16 v[72:75], a[80:83], v[136:139], v[64:67]\n" \
"v_mfma_f32_16x16x32_bf16 v[40:43], a[60:63], v[224:227], v[40:43]\n" \
"ds_read_b64_tr_b16 a[56:57], v108 offset:16384\n" \
"ds_read_b64_tr_b16 a[58:59], v108 offset:16896\n" \
"buffer_load_dwordx4 v2, s[44:47], s59 offen lds\n" \
"v_mfma_f32_16x16x32_bf16 v[76:79], a[84:87], v[136:139], v[64:67]\n" \
"v_mfma_f32_16x16x32_bf16 v[44:47], a[60:63], v[228:231], v[44:47]\n" \
"v_cvt_pk_bf16_f32 v128, v80, v81\n" \
"v_cvt_pk_bf16_f32 v129, v82, v83\n" \
"v_cvt_pk_bf16_f32 v130, v84, v85\n" \
"v_cvt_pk_bf16_f32 v131, v86, v87\n" \
"s_waitcnt lgkmcnt(6)\n" \
"v_mfma_f32_16x16x32_bf16 v[16:19], a[64:67], v[232:235], v[16:19]\n" \
"ds_read_b64_tr_b16 a[60:61], v109 offset:16384\n" \
"ds_read_b64_tr_b16 a[62:63], v109 offset:16896\n" \
"s_add_u32 s59, s56, 3539072\n" \
"s_add_u32 m0, s58, 122880\n" \
"v_mfma_f32_16x16x32_bf16 v[20:23], a[64:67], v[236:239], v[20:23]\n" \
"v_accvgpr_write_b32 a16, v128\n" \
"v_accvgpr_write_b32 a17, v129\n" \
"v_accvgpr_write_b32 a18, v130\n" \
"v_accvgpr_write_b32 a19, v131\n" \
"v_mfma_f32_16x16x32_bf16 v[80:83], a[80:83], v[140:143], v[68:71]\n" \
"v_mfma_f32_16x16x32_bf16 v[24:27], a[68:71], v[232:235], v[24:27]\n" \
"ds_read_b64_tr_b16 a[64:65], v106 offset:20480\n" \
"ds_read_b64_tr_b16 a[66:67], v106 offset:20992\n" \
"buffer_load_dwordx4 v2, s[44:47], s59 offen lds\n" \
"v_mfma_f32_16x16x32_bf16 v[84:87], a[84:87], v[140:143], v[68:71]\n" \
"v_mfma_f32_16x16x32_bf16 v[28:31], a[68:71], v[236:239], v[28:31]\n" \
"v_cvt_pk_bf16_f32 v132, v88, v89\n" \
"v_cvt_pk_bf16_f32 v133, v90, v91\n" \
"v_cvt_pk_bf16_f32 v134, v92, v93\n" \
"v_cvt_pk_bf16_f32 v135, v94, v95\n" \
"v_mfma_f32_16x16x32_bf16 v[32:35], a[72:75], v[232:235], v[32:35]\n" \
"ds_read_b64_tr_b16 a[68:69], v107 offset:20480\n" \
"ds_read_b64_tr_b16 a[70:71], v107 offset:20992\n" \
"s_add_u32 s59, s56, 3833984\n" \
"s_add_u32 m0, s58, 126976\n" \
"v_mfma_f32_16x16x32_bf16 v[36:39], a[72:75], v[236:239], v[36:39]\n" \
"v_accvgpr_write_b32 a20, v132\n" \
"v_accvgpr_write_b32 a21, v133\n" \
"v_accvgpr_write_b32 a22, v134\n" \
"v_accvgpr_write_b32 a23, v135\n" \
"v_mfma_f32_16x16x32_bf16 v[88:91], a[80:83], v[144:147], v[64:67]\n" \
"v_mfma_f32_16x16x32_bf16 v[40:43], a[76:79], v[232:235], v[40:43]\n" \
"ds_read_b64_tr_b16 a[72:73], v108 offset:20480\n" \
"ds_read_b64_tr_b16 a[74:75], v108 offset:20992\n" \
"buffer_load_dwordx4 v2, s[44:47], s59 offen lds\n" \
"v_mfma_f32_16x16x32_bf16 v[92:95], a[84:87], v[144:147], v[64:67]\n" \
"v_mfma_f32_16x16x32_bf16 v[44:47], a[76:79], v[236:239], v[44:47]\n" \
"v_cvt_pk_bf16_f32 v136, v72, v73\n" \
"v_cvt_pk_bf16_f32 v137, v74, v75\n" \
"v_cvt_pk_bf16_f32 v138, v76, v77\n" \
"v_cvt_pk_bf16_f32 v139, v78, v79\n" \
"s_waitcnt lgkmcnt(6)\n" \
"v_mfma_f32_16x16x32_bf16 v[16:19], a[48:51], v[240:243], v[16:19]\n" \
"ds_read_b64_tr_b16 a[76:77], v109 offset:20480\n" \
"ds_read_b64_tr_b16 a[78:79], v109 offset:20992\n" \
"s_waitcnt vmcnt(14)\n" \
"s_barrier\n" \
"s_add_u32 s59, s56, 4128896\n" \
"s_add_u32 m0, s58, 131072\n" \
"v_mfma_f32_16x16x32_bf16 v[20:23], a[48:51], v[244:247], v[20:23]\n" \
"v_accvgpr_write_b32 a24, v136\n" \
"v_accvgpr_write_b32 a25, v137\n" \
"v_accvgpr_write_b32 a26, v138\n" \
"v_accvgpr_write_b32 a27, v139\n" \
"v_mfma_f32_16x16x32_bf16 v[72:75], a[80:83], v[148:151], v[68:71]\n" \
"v_mfma_f32_16x16x32_bf16 v[24:27], a[52:55], v[240:243], v[24:27]\n" \
"ds_read_b64_tr_b16 a[48:49], v4 offset:0\n" \
"ds_read_b64_tr_b16 a[50:51], v4 offset:512\n" \
"buffer_load_dwordx4 v2, s[44:47], s59 offen lds\n" \
"v_mfma_f32_16x16x32_bf16 v[76:79], a[84:87], v[148:151], v[68:71]\n" \
"v_mfma_f32_16x16x32_bf16 v[28:31], a[52:55], v[244:247], v[28:31]\n" \
"v_cvt_pk_bf16_f32 v140, v80, v81\n" \
"v_cvt_pk_bf16_f32 v141, v82, v83\n" \
"v_cvt_pk_bf16_f32 v142, v84, v85\n" \
"v_cvt_pk_bf16_f32 v143, v86, v87\n" \
"v_mfma_f32_16x16x32_bf16 v[32:35], a[56:59], v[240:243], v[32:35]\n" \
"ds_read_b64_tr_b16 a[52:53], v5 offset:0\n" \
"ds_read_b64_tr_b16 a[54:55], v5 offset:512\n" \
"s_add_u32 s59, s56, 4423808\n" \
"s_add_u32 m0, s58, 135168\n" \
"v_mfma_f32_16x16x32_bf16 v[36:39], a[56:59], v[244:247], v[36:39]\n" \
"v_accvgpr_write_b32 a28, v140\n" \
"v_accvgpr_write_b32 a29, v141\n" \
"v_accvgpr_write_b32 a30, v142\n" \
"v_accvgpr_write_b32 a31, v143\n" \
"v_mfma_f32_16x16x32_bf16 v[80:83], a[80:83], v[152:155], v[64:67]\n" \
"v_mfma_f32_16x16x32_bf16 v[40:43], a[60:63], v[240:243], v[40:43]\n" \
"ds_read_b64_tr_b16 a[56:57], v4 offset:4096\n" \
"ds_read_b64_tr_b16 a[58:59], v4 offset:4608\n" \
"buffer_load_dwordx4 v2, s[44:47], s59 offen lds\n" \
"v_mfma_f32_16x16x32_bf16 v[84:87], a[84:87], v[152:155], v[64:67]\n" \
"v_mfma_f32_16x16x32_bf16 v[44:47], a[60:63], v[244:247], v[44:47]\n" \
"v_cvt_pk_bf16_f32 v144, v88, v89\n" \
"v_cvt_pk_bf16_f32 v145, v90, v91\n" \
"v_cvt_pk_bf16_f32 v146, v92, v93\n" \
"v_cvt_pk_bf16_f32 v147, v94, v95\n" \
"s_waitcnt lgkmcnt(6)\n" \
"v_mfma_f32_16x16x32_bf16 v[16:19], a[64:67], v[248:251], v[16:19]\n" \
"ds_read_b64_tr_b16 a[60:61], v5 offset:4096\n" \
"ds_read_b64_tr_b16 a[62:63], v5 offset:4608\n" \
"s_add_u32 s59, s56, 4718720\n" \
"s_add_u32 m0, s58, 139264\n" \
"v_mfma_f32_16x16x32_bf16 v[20:23], a[64:67], v[252:255], v[20:23]\n" \
"v_accvgpr_write_b32 a32, v144\n" \
"v_accvgpr_write_b32 a33, v145\n" \
"v_accvgpr_write_b32 a34, v146\n" \
"v_accvgpr_write_b32 a35, v147\n" \
"v_mfma_f32_16x16x32_bf16 v[88:91], a[80:83], v[156:159], v[68:71]\n" \
"v_mfma_f32_16x16x32_bf16 v[24:27], a[68:71], v[248:251], v[24:27]\n" \
"ds_read_b64_tr_b16 a[64:65], v4 offset:8192\n" \
"ds_read_b64_tr_b16 a[66:67], v4 offset:8704\n" \
"buffer_load_dwordx4 v2, s[44:47], s59 offen lds\n" \
"v_mfma_f32_16x16x32_bf16 v[92:95], a[84:87], v[156:159], v[68:71]\n" \
"v_mfma_f32_16x16x32_bf16 v[28:31], a[68:71], v[252:255], v[28:31]\n" \
"v_cvt_pk_bf16_f32 v148, v72, v73\n" \
"v_cvt_pk_bf16_f32 v149, v74, v75\n" \
"v_cvt_pk_bf16_f32 v150, v76, v77\n" \
"v_cvt_pk_bf16_f32 v151, v78, v79\n" \
"v_mfma_f32_16x16x32_bf16 v[32:35], a[72:75], v[248:251], v[32:35]\n" \
"ds_read_b64_tr_b16 a[68:69], v5 offset:8192\n" \
"ds_read_b64_tr_b16 a[70:71], v5 offset:8704\n" \
"s_add_u32 s59, s56, 5013632\n" \
"s_add_u32 m0, s58, 143360\n" \
"v_accvgpr_write_b32 a36, v148\n" \
"v_mfma_f32_16x16x32_bf16 v[36:39], a[72:75], v[252:255], v[36:39]\n" \
"v_accvgpr_write_b32 a37, v149\n" \
"v_accvgpr_write_b32 a38, v150\n" \
"v_accvgpr_write_b32 a39, v151\n" \
"s_nop 1\n" \
"v_cvt_pk_bf16_f32 v152, v80, v81\n" \
"v_mfma_f32_16x16x32_bf16 v[40:43], a[76:79], v[248:251], v[40:43]\n" \
"ds_read_b64_tr_b16 a[72:73], v4 offset:12288\n" \
"ds_read_b64_tr_b16 a[74:75], v4 offset:12800\n" \
"buffer_load_dwordx4 v2, s[44:47], s59 offen lds\n" \
"v_cvt_pk_bf16_f32 v153, v82, v83\n" \
"v_mfma_f32_16x16x32_bf16 v[44:47], a[76:79], v[252:255], v[44:47]\n" \
"v_cvt_pk_bf16_f32 v154, v84, v85\n" \
"v_cvt_pk_bf16_f32 v155, v86, v87\n" \
"v_accvgpr_write_b32 a40, v152\n" \
"v_accvgpr_write_b32 a41, v153\n" \
"v_accvgpr_write_b32 a42, v154\n" \
"s_waitcnt lgkmcnt(6)\n" \
"v_mfma_f32_16x16x32_bf16 v[16:19], a[48:51], a[0:3], v[16:19]\n" \
"ds_read_b64_tr_b16 a[76:77], v5 offset:12288\n" \
"ds_read_b64_tr_b16 a[78:79], v5 offset:12800\n" \
"v_accvgpr_write_b32 a43, v155\n" \
"s_nop 1\n" \
"v_mfma_f32_16x16x32_bf16 v[20:23], a[48:51], a[4:7], v[20:23]\n" \
"v_cvt_pk_bf16_f32 v156, v88, v89\n" \
"v_cvt_pk_bf16_f32 v157, v90, v91\n" \
"v_cvt_pk_bf16_f32 v158, v92, v93\n" \
"v_cvt_pk_bf16_f32 v159, v94, v95\n" \
"v_mfma_f32_16x16x32_bf16 v[24:27], a[52:55], a[0:3], v[24:27]\n" \
"ds_read_b64_tr_b16 a[48:49], v4 offset:16384\n" \
"ds_read_b64_tr_b16 a[50:51], v4 offset:16896\n" \
"s_add_u32 s59, s56, 256\n" \
"s_add_u32 m0, s58, 0\n" \
"v_mfma_f32_16x16x32_bf16 v[28:31], a[52:55], a[4:7], v[28:31]\n" \
"v_accvgpr_write_b32 a44, v156\n" \
"v_accvgpr_write_b32 a45, v157\n" \
"v_accvgpr_write_b32 a46, v158\n" \
"v_accvgpr_write_b32 a47, v159\n" \
"v_mfma_f32_16x16x32_bf16 v[16:19], a[56:59], a[8:11], v[16:19]\n" \
"ds_read_b64_tr_b16 a[52:53], v5 offset:16384\n" \
"ds_read_b64_tr_b16 a[54:55], v5 offset:16896\n" \
"v_mfma_f32_16x16x32_bf16 v[20:23], a[56:59], a[12:15], v[20:23]\n" \
"buffer_load_dwordx4 v2, s[44:47], s59 offen lds\n" \
"v_mfma_f32_16x16x32_bf16 v[24:27], a[60:63], a[8:11], v[24:27]\n" \
"ds_read_b64_tr_b16 a[56:57], v4 offset:20480\n" \
"ds_read_b64_tr_b16 a[58:59], v4 offset:20992\n" \
"v_mfma_f32_16x16x32_bf16 v[28:31], a[60:63], a[12:15], v[28:31]\n" \
"s_add_u32 s59, s56, 295168\n" \
"s_add_u32 m0, s58, 4096\n" \
"s_waitcnt lgkmcnt(6)\n" \
"v_mfma_f32_16x16x32_bf16 v[16:19], a[64:67], a[16:19], v[16:19]\n" \
"ds_read_b64_tr_b16 a[60:61], v5 offset:20480\n" \
"ds_read_b64_tr_b16 a[62:63], v5 offset:20992\n" \
"v_mfma_f32_16x16x32_bf16 v[20:23], a[64:67], a[20:23], v[20:23]\n" \
"buffer_load_dwordx4 v2, s[44:47], s59 offen lds\n" \
"v_mfma_f32_16x16x32_bf16 v[24:27], a[68:71], a[16:19], v[24:27]\n" \
"ds_read_b64_tr_b16 a[64:65], v11 offset:0\n" \
"ds_read_b64_tr_b16 a[66:67], v11 offset:512\n" \
"v_mfma_f32_16x16x32_bf16 v[28:31], a[68:71], a[20:23], v[28:31]\n" \
"s_add_u32 s59, s56, 590080\n" \
"s_add_u32 m0, s58, 8192\n" \
"v_mfma_f32_16x16x32_bf16 v[16:19], a[72:75], a[24:27], v[16:19]\n" \
"ds_read_b64_tr_b16 a[68:69], v0 offset:0\n" \
"ds_read_b64_tr_b16 a[70:71], v0 offset:512\n" \
"v_mfma_f32_16x16x32_bf16 v[20:23], a[72:75], a[28:31], v[20:23]\n" \
"buffer_load_dwordx4 v2, s[44:47], s59 offen lds\n" \
"v_add_u32 v4, 73728, v6\n" \
"v_mfma_f32_16x16x32_bf16 v[24:27], a[76:79], a[24:27], v[24:27]\n" \
"ds_read_b64_tr_b16 a[72:73], v11 offset:4096\n" \
"ds_read_b64_tr_b16 a[74:75], v11 offset:4608\n" \
"v_mfma_f32_16x16x32_bf16 v[28:31], a[76:79], a[28:31], v[28:31]\n" \
"s_add_u32 s59, s56, 884992\n" \
"s_add_u32 m0, s58, 12288\n" \
"s_waitcnt lgkmcnt(6)\n" \
"v_mfma_f32_16x16x32_bf16 v[16:19], a[48:51], a[32:35], v[16:19]\n" \
"ds_read_b64_tr_b16 a[76:77], v0 offset:4096\n" \
"ds_read_b64_tr_b16 a[78:79], v0 offset:4608\n" \
"v_mfma_f32_16x16x32_bf16 v[20:23], a[48:51], a[36:39], v[20:23]\n" \
"buffer_load_dwordx4 v2, s[44:47], s59 offen lds\n" \
"v_xor_b32 v5, 32, v4\n" \
"v_mfma_f32_16x16x32_bf16 v[24:27], a[52:55], a[32:35], v[24:27]\n" \
"ds_read_b64_tr_b16 a[48:49], v11 offset:8192\n" \
"ds_read_b64_tr_b16 a[50:51], v11 offset:8704\n" \
"v_mfma_f32_16x16x32_bf16 v[28:31], a[52:55], a[36:39], v[28:31]\n" \
"s_add_u32 s59, s56, 1179904\n" \
"s_add_u32 m0, s58, 16384\n" \
"v_mfma_f32_16x16x32_bf16 v[16:19], a[56:59], a[40:43], v[16:19]\n" \
"ds_read_b64_tr_b16 a[52:53], v0 offset:8192\n" \
"ds_read_b64_tr_b16 a[54:55], v0 offset:8704\n" \
"v_mfma_f32_16x16x32_bf16 v[20:23], a[56:59], a[44:47], v[20:23]\n" \
"buffer_load_dwordx4 v2, s[44:47], s59 offen lds\n" \
"v_mfma_f32_16x16x32_bf16 v[24:27], a[60:63], a[40:43], v[24:27]\n" \
"ds_read_b64_tr_b16 a[56:57], v11 offset:12288\n" \
"ds_read_b64_tr_b16 a[58:59], v11 offset:12800\n" \
"v_mfma_f32_16x16x32_bf16 v[28:31], a[60:63], a[44:47], v[28:31]\n" \
"s_add_u32 s59, s56, 1474816\n" \
"s_add_u32 m0, s58, 20480\n" \
"s_waitcnt lgkmcnt(6)\n" \
"v_mfma_f32_16x16x32_bf16 v[32:35], a[64:67], a[0:3], v[32:35]\n" \
"ds_read_b64_tr_b16 a[60:61], v0 offset:12288\n" \
"ds_read_b64_tr_b16 a[62:63], v0 offset:12800\n" \
"v_mfma_f32_16x16x32_bf16 v[36:39], a[64:67], a[4:7], v[36:39]\n" \
"buffer_load_dwordx4 v2, s[44:47], s59 offen lds\n" \
"v_mfma_f32_16x16x32_bf16 v[40:43], a[68:71], a[0:3], v[40:43]\n" \
"ds_read_b64_tr_b16 a[64:65], v11 offset:16384\n" \
"ds_read_b64_tr_b16 a[66:67], v11 offset:16896\n" \
"v_mfma_f32_16x16x32_bf16 v[44:47], a[68:71], a[4:7], v[44:47]\n" \
"v_mfma_f32_16x16x32_bf16 v[32:35], a[72:75], a[8:11], v[32:35]\n" \
"ds_read_b64_tr_b16 a[68:69], v0 offset:16384\n" \
"ds_read_b64_tr_b16 a[70:71], v0 offset:16896\n" \
"v_mfma_f32_16x16x32_bf16 v[36:39], a[72:75], a[12:15], v[36:39]\n" \
"v_cvt_pk_bf16_f32 v48, v16, v17\n" \
"v_cvt_pk_bf16_f32 v49, v18, v19\n" \
"v_mfma_f32_16x16x32_bf16 v[40:43], a[76:79], a[8:11], v[40:43]\n" \
"ds_read_b64_tr_b16 a[72:73], v11 offset:20480\n" \
"ds_read_b64_tr_b16 a[74:75], v11 offset:20992\n" \
"v_mfma_f32_16x16x32_bf16 v[44:47], a[76:79], a[12:15], v[44:47]\n" \
"v_cvt_pk_bf16_f32 v50, v24, v25\n" \
"s_waitcnt lgkmcnt(6)\n" \
"v_mfma_f32_16x16x32_bf16 v[32:35], a[48:51], a[16:19], v[32:35]\n" \
"ds_read_b64_tr_b16 a[76:77], v0 offset:20480\n" \
"ds_read_b64_tr_b16 a[78:79], v0 offset:20992\n" \
"v_mfma_f32_16x16x32_bf16 v[36:39], a[48:51], a[20:23], v[36:39]\n" \
"s_waitcnt vmcnt(18)\n" \
"s_barrier\n" \
"v_mfma_f32_16x16x32_bf16 v[40:43], a[52:55], a[16:19], v[40:43]\n" \
"ds_read_b64_tr_b16 a[48:49], v4 offset:0\n" \
"ds_read_b64_tr_b16 a[50:51], v4 offset:512\n" \
"v_mfma_f32_16x16x32_bf16 v[44:47], a[52:55], a[20:23], v[44:47]\n" \
"v_cvt_pk_bf16_f32 v51, v26, v27\n" \
"v_cvt_pk_bf16_f32 v52, v20, v21\n" \
"v_mfma_f32_16x16x32_bf16 v[32:35], a[56:59], a[24:27], v[32:35]\n" \
"ds_read_b64_tr_b16 a[52:53], v5 offset:0\n" \
"ds_read_b64_tr_b16 a[54:55], v5 offset:512\n" \
"v_mfma_f32_16x16x32_bf16 v[36:39], a[56:59], a[28:31], v[36:39]\n" \
"v_cvt_pk_bf16_f32 v53, v22, v23\n" \
"v_cvt_pk_bf16_f32 v54, v28, v29\n" \
"v_mfma_f32_16x16x32_bf16 v[40:43], a[60:63], a[24:27], v[40:43]\n" \
"ds_read_b64_tr_b16 a[56:57], v4 offset:4096\n" \
"ds_read_b64_tr_b16 a[58:59], v4 offset:4608\n" \
"v_mfma_f32_16x16x32_bf16 v[44:47], a[60:63], a[28:31], v[44:47]\n" \
"v_cvt_pk_bf16_f32 v55, v30, v31\n" \
"s_waitcnt lgkmcnt(6)\n" \
"v_mfma_f32_16x16x32_bf16 v[32:35], a[64:67], a[32:35], v[32:35]\n" \
"ds_read_b64_tr_b16 a[60:61], v5 offset:4096\n" \
"ds_read_b64_tr_b16 a[62:63], v5 offset:4608\n" \
"v_mfma_f32_16x16x32_bf16 v[36:39], a[64:67], a[36:39], v[36:39]\n" \
"s_add_u32 s59, s56, 1769728\n" \
"v_mfma_f32_16x16x32_bf16 v[40:43], a[68:71], a[32:35], v[40:43]\n" \
"ds_read_b64_tr_b16 a[64:65], v4 offset:8192\n" \
"ds_read_b64_tr_b16 a[66:67], v4 offset:8704\n" \
"v_mfma_f32_16x16x32_bf16 v[44:47], a[68:71], a[36:39], v[44:47]\n" \
"v_mfma_f32_16x16x32_bf16 v[32:35], a[72:75], a[40:43], v[32:35]\n" \
"ds_read_b64_tr_b16 a[68:69], v5 offset:8192\n" \
"ds_read_b64_tr_b16 a[70:71], v5 offset:8704\n" \
"v_mfma_f32_16x16x32_bf16 v[36:39], a[72:75], a[44:47], v[36:39]\n" \
"v_mfma_f32_16x16x32_bf16 v[40:43], a[76:79], a[40:43], v[40:43]\n" \
"ds_read_b64_tr_b16 a[72:73], v4 offset:12288\n" \
"ds_read_b64_tr_b16 a[74:75], v4 offset:12800\n" \
"v_mfma_f32_16x16x32_bf16 v[44:47], a[76:79], a[44:47], v[44:47]\n" \
"L1_%=:\n" \
"s_waitcnt lgkmcnt(6)\n" \
"v_mfma_f32_16x16x32_bf16 v[16:19], a[48:51], v[160:163], 0\n" \
"ds_read_b64_tr_b16 a[76:77], v5 offset:12288\n" \
"ds_read_b64_tr_b16 a[78:79], v5 offset:12800\n" \
"v_mfma_f32_16x16x32_bf16 v[20:23], a[48:51], v[164:167], 0\n" \
"v_xor_b32 v11, 64, v4\n" \
"v_xor_b32 v0, 96, v4\n" \
"v_mfma_f32_16x16x32_bf16 v[24:27], a[52:55], v[160:163], 0\n" \
"ds_read_b64_tr_b16 a[48:49], v4 offset:16384\n" \
"ds_read_b64_tr_b16 a[50:51], v4 offset:16896\n" \
"v_mfma_f32_16x16x32_bf16 v[28:31], a[52:55], v[164:167], 0\n" \
"s_add_u32 m0, s69, 24576\n" \
"v_cvt_pk_bf16_f32 v56, v32, v33\n" \
"v_mfma_f32_16x16x32_bf16 v[16:19], a[56:59], v[168:171], v[16:19]\n" \
"ds_read_b64_tr_b16 a[52:53], v5 offset:16384\n" \
"ds_read_b64_tr_b16 a[54:55], v5 offset:16896\n" \
"v_mfma_f32_16x16x32_bf16 v[20:23], a[56:59], v[172:175], v[20:23]\n" \
"buffer_load_dwordx4 v2, s[44:47], s59 offen lds\n" \
"s_add_u32 s59, s59, s66\n" \
"v_mfma_f32_16x16x32_bf16 v[24:27], a[60:63], v[168:171], v[24:27]\n" \
"ds_read_b64_tr_b16 a[56:57], v4 offset:20480\n" \
"ds_read_b64_tr_b16 a[58:59], v4 offset:20992\n" \
"v_mfma_f32_16x16x32_bf16 v[28:31], a[60:63], v[172:175], v[28:31]\n" \
"v_cvt_pk_bf16_f32 v57, v34, v35\n" \
"s_waitcnt lgkmcnt(6)\n" \
"v_mfma_f32_16x16x32_bf16 v[16:19], a[64:67], v[176:179], v[16:19]\n" \
"ds_read_b64_tr_b16 a[60:61], v5 offset:20480\n" \
"ds_read_b64_tr_b16 a[62:63], v5 offset:20992\n" \
"v_mfma_f32_16x16x32_bf16 v[20:23], a[64:67], v[180:183], v[20:23]\n" \
"v_cvt_pk_bf16_f32 v58, v40, v41\n" \
"v_cvt_pk_bf16_f32 v59, v42, v43\n" \
"v_mfma_f32_16x16x32_bf16 v[24:27], a[68:71], v[176:179], v[24:27]\n" \
"ds_read_b64_tr_b16 a[64:65], v11 offset:0\n" \
"ds_read_b64_tr_b16 a[66:67], v11 offset:512\n" \
"v_mfma_f32_16x16x32_bf16 v[28:31], a[68:71], v[180:183], v[28:31]\n" \
"s_add_u32 m0, s69, 28672\n" \
"v_add_u32 v4, 24576, v7\n" \
"v_mfma_f32_16x16x32_bf16 v[16:19], a[72:75], v[184:187], v[16:19]\n" \
"ds_read_b64_tr_b16 a[68:69], v0 offset:0\n" \
"ds_read_b64_tr_b16 a[70:71], v0 offset:512\n" \
"v_mfma_f32_16x16x32_bf16 v[20:23], a[72:75], v[188:191], v[20:23]\n" \
"buffer_load_dwordx4 v2, s[44:47], s59 offen lds\n" \
"s_add_u32 s59, s59, s66\n" \
"v_mfma_f32_16x16x32_bf16 v[24:27], a[76:79], v[184:187], v[24:27]\n" \
"ds_read_b64_tr_b16 a[72:73], v11 offset:4096\n" \
"ds_read_b64_tr_b16 a[74:75], v11 offset:4608\n" \
"v_mfma_f32_16x16x32_bf16 v[28:31], a[76:79], v[188:191], v[28:31]\n" \
"v_xor_b32 v5, 32, v4\n" \
"s_waitcnt lgkmcnt(6)\n" \
"v_mfma_f32_16x16x32_bf16 v[16:19], a[48:51], v[192:195], v[16:19]\n" \
"ds_read_b64_tr_b16 a[76:77], v0 offset:4096\n" \
"ds_read_b64_tr_b16 a[78:79], v0 offset:4608\n" \
"v_mfma_f32_16x16x32_bf16 v[20:23], a[48:51], v[196:199], v[20:23]\n" \
"v_cvt_pk_bf16_f32 v60, v36, v37\n" \
"v_cvt_pk_bf16_f32 v61, v38, v39\n" \
"v_mfma_f32_16x16x32_bf16 v[24:27], a[52:55], v[192:195], v[24:27]\n" \
"ds_read_b64_tr_b16 a[48:49], v11 offset:8192\n" \
"ds_read_b64_tr_b16 a[50:51], v11 offset:8704\n" \
"v_mfma_f32_16x16x32_bf16 v[28:31], a[52:55], v[196:199], v[28:31]\n" \
"s_add_u32 m0, s69, 32768\n" \
"v_cvt_pk_bf16_f32 v62, v44, v45\n" \
"v_mfma_f32_16x16x32_bf16 v[16:19], a[56:59], v[200:203], v[16:19]\n" \
"ds_read_b64_tr_b16 a[52:53], v0 offset:8192\n" \
"ds_read_b64_tr_b16 a[54:55], v0 offset:8704\n" \
"v_mfma_f32_16x16x32_bf16 v[20:23], a[56:59], v[204:207], v[20:23]\n" \
"buffer_load_dwordx4 v2, s[44:47], s59 offen lds\n" \
"s_add_u32 s59, s59, s66\n" \
"v_mfma_f32_16x16x32_bf16 v[24:27], a[60:63], v[200:203], v[24:27]\n" \
"ds_read_b64_tr_b16 a[56:57], v11 offset:12288\n" \
"ds_read_b64_tr_b16 a[58:59], v11 offset:12800\n" \
"v_mfma_f32_16x16x32_bf16 v[28:31], a[60:63], v[204:207], v[28:31]\n" \
"v_cvt_pk_bf16_f32 v63, v46, v47\n" \
"s_waitcnt lgkmcnt(6)\n" \
"v_mfma_f32_16x16x32_bf16 v[32:35], a[64:67], v[160:163], 0\n" \
"ds_read_b64_tr_b16 a[60:61], v0 offset:12288\n" \
"ds_read_b64_tr_b16 a[62:63], v0 offset:12800\n" \
"v_mfma_f32_16x16x32_bf16 v[36:39], a[64:67], v[164:167], 0\n" \
"s_add_u32 m0, s69, 36864\n" \
"v_mfma_f32_16x16x32_bf16 v[40:43], a[68:71], v[160:163], 0\n" \
"ds_read_b64_tr_b16 a[64:65], v11 offset:16384\n" \
"ds_read_b64_tr_b16 a[66:67], v11 offset:16896\n" \
"v_mfma_f32_16x16x32_bf16 v[44:47], a[68:71], v[164:167], 0\n" \
"buffer_load_dwordx4 v2, s[44:47], s59 offen lds\n" \
"s_add_u32 s59, s59, s66\n" \
"v_mfma_f32_16x16x32_bf16 v[32:35], a[72:75], v[168:171], v[32:35]\n" \
"ds_read_b64_tr_b16 a[68:69], v0 offset:16384\n" \
"ds_read_b64_tr_b16 a[70:71], v0 offset:16896\n" \
"v_mfma_f32_16x16x32_bf16 v[36:39], a[72:75], v[172:175], v[36:39]\n" \
"s_set_gpr_idx_on s73, gpr_idx(DST)\n" \
"v_pk_max_i16 v64, v48, 0\n" \
"v_pk_max_i16 v65, v49, 0\n" \
"v_pk_max_i16 v66, v50, 0\n" \
"v_pk_max_i16 v67, v51, 0\n" \
"v_pk_max_i16 v68, v52, 0\n" \
"v_pk_max_i16 v69, v53, 0\n" \
"v_pk_max_i16 v70, v54, 0\n" \
"v_pk_max_i16 v71, v55, 0\n" \
"s_set_gpr_idx_off\n" \
"v_mfma_f32_16x16x32_bf16 v[40:43], a[76:79], v[168:171], v[40:43]\n" \
"ds_read_b64_tr_b16 a[72:73], v11 offset:20480\n" \
"ds_read_b64_tr_b16 a[74:75], v11 offset:20992\n" \
"v_mfma_f32_16x16x32_bf16 v[44:47], a[76:79], v[172:175], v[44:47]\n" \
"s_waitcnt lgkmcnt(6)\n" \
"v_mfma_f32_16x16x32_bf16 v[32:35], a[48:51], v[176:179], v[32:35]\n" \
"ds_read_b64_tr_b16 a[76:77], v0 offset:20480\n" \
"ds_read_b64_tr_b16 a[78:79], v0 offset:20992\n" \
"v_mfma_f32_16x16x32_bf16 v[36:39], a[48:51], v[180:183], v[36:39]\n" \
"s_waitcnt vmcnt(16)\n" \
"s_barrier\n" \
"v_mfma_f32_16x16x32_bf16 v[40:43], a[52:55], v[176:179], v[40:43]\n" \
"ds_read_b64_tr_b16 a[48:49], v4 offset:0\n" \
"ds_read_b64_tr_b16 a[50:51], v4 offset:512\n" \
"v_mfma_f32_16x16x32_bf16 v[44:47], a[52:55], v[180:183], v[44:47]\n" \
"v_xor_b32 v11, 64, v4\n" \
"v_xor_b32 v0, 96, v4\n" \
"v_mfma_f32_16x16x32_bf16 v[32:35], a[56:59], v[184:187], v[32:35]\n" \
"ds_read_b64_tr_b16 a[52:53], v5 offset:0\n" \
"ds_read_b64_tr_b16 a[54:55], v5 offset:512\n" \
"v_mfma_f32_16x16x32_bf16 v[36:39], a[56:59], v[188:191], v[36:39]\n" \
"s_add_u32 m0, s69, 40960\n" \
"v_mfma_f32_16x16x32_bf16 v[40:43], a[60:63], v[184:187], v[40:43]\n" \
"ds_read_b64_tr_b16 a[56:57], v4 offset:4096\n" \
"ds_read_b64_tr_b16 a[58:59], v4 offset:4608\n" \
"v_mfma_f32_16x16x32_bf16 v[44:47], a[60:63], v[188:191], v[44:47]\n" \
"buffer_load_dwordx4 v2, s[44:47], s59 offen lds\n" \
"s_add_u32 s59, s59, s66\n" \
"s_waitcnt lgkmcnt(6)\n" \
"v_mfma_f32_16x16x32_bf16 v[32:35], a[64:67], v[192:195], v[32:35]\n" \
"ds_read_b64_tr_b16 a[60:61], v5 offset:4096\n" \
"ds_read_b64_tr_b16 a[62:63], v5 offset:4608\n" \
"v_mfma_f32_16x16x32_bf16 v[36:39], a[64:67], v[196:199], v[36:39]\n" \
"s_set_gpr_idx_on s73, gpr_idx(DST)\n" \
"v_pk_max_i16 v72, v56, 0\n" \
"v_pk_max_i16 v73, v57, 0\n" \
"v_pk_max_i16 v74, v58, 0\n" \
"v_pk_max_i16 v75, v59, 0\n" \
"v_pk_max_i16 v76, v60, 0\n" \
"v_pk_max_i16 v77, v61, 0\n" \
"v_pk_max_i16 v78, v62, 0\n" \
"v_pk_max_i16 v79, v63, 0\n" \
"s_set_gpr_idx_off\n" \
"v_mfma_f32_16x16x32_bf16 v[40:43], a[68:71], v[192:195], v[40:43]\n" \
"ds_read_b64_tr_b16 a[64:65], v4 offset:8192\n" \
"ds_read_b64_tr_b16 a[66:67], v4 offset:8704\n" \
"v_mfma_f32_16x16x32_bf16 v[44:47], a[68:71], v[196:199], v[44:47]\n" \
"s_add_u32 m0, s69, 45056\n" \
"s_add_u32 s73, s73, 16\n" \
"v_mfma_f32_16x16x32_bf16 v[32:35], a[72:75], v[200:203], v[32:35]\n" \
"ds_read_b64_tr_b16 a[68:69], v5 offset:8192\n" \
"ds_read_b64_tr_b16 a[70:71], v5 offset:8704\n" \
"v_mfma_f32_16x16x32_bf16 v[36:39], a[72:75], v[204:207], v[36:39]\n" \
"buffer_load_dwordx4 v2, s[44:47], s59 offen lds\n" \
"s_add_u32 s59, s59, s66\n" \
"v_mfma_f32_16x16x32_bf16 v[40:43], a[76:79], v[200:203], v[40:43]\n" \
"ds_read_b64_tr_b16 a[72:73], v4 offset:12288\n" \
"ds_read_b64_tr_b16 a[74:75], v4 offset:12800\n" \
"v_mfma_f32_16x16x32_bf16 v[44:47], a[76:79], v[204:207], v[44:47]\n" \
"s_waitcnt lgkmcnt(6)\n" \
"v_mfma_f32_16x16x32_bf16 v[16:19], a[48:51], v[208:211], v[16:19]\n" \
"ds_read_b64_tr_b16 a[76:77], v5 offset:12288\n" \
"ds_read_b64_tr_b16 a[78:79], v5 offset:12800\n" \
"v_mfma_f32_16x16x32_bf16 v[20:23], a[48:51], v[212:215], v[20:23]\n" \
"s_add_u32 s59, s59, s67\n" \
"v_mfma_f32_16x16x32_bf16 v[24:27], a[52:55], v[208:211], v[24:27]\n" \
"ds_read_b64_tr_b16 a[48:49], v4 offset:16384\n" \
"ds_read_b64_tr_b16 a[50:51], v4 offset:16896\n" \
"v_mfma_f32_16x16x32_bf16 v[28:31], a[52:55], v[212:215], v[28:31]\n" \
"s_add_u32 m0, s69, 49152\n" \
"v_mfma_f32_16x16x32_bf16 v[16:19], a[56:59], v[216:219], v[16:19]\n" \
"ds_read_b64_tr_b16 a[52:53], v5 offset:16384\n" \
"ds_read_b64_tr_b16 a[54:55], v5 offset:16896\n" \
"v_mfma_f32_16x16x32_bf16 v[20:23], a[56:59], v[220:223], v[20:23]\n" \
"buffer_load_dwordx4 v2, s[44:47], s59 offen lds\n" \
"s_add_u32 s59, s59, s66\n" \
"v_mfma_f32_16x16x32_bf16 v[24:27], a[60:63], v[216:219], v[24:27]\n" \
"ds_read_b64_tr_b16 a[56:57], v4 offset:20480\n" \
"ds_read_b64_tr_b16 a[58:59], v4 offset:20992\n" \
"v_mfma_f32_16x16x32_bf16 v[28:31], a[60:63], v[220:223], v[28:31]\n" \
"s_waitcnt lgkmcnt(6)\n" \
"v_mfma_f32_16x16x32_bf16 v[16:19], a[64:67], v[224:227], v[16:19]\n" \
"ds_read_b64_tr_b16 a[60:61], v5 offset:20480\n" \
"ds_read_b64_tr_b16 a[62:63], v5 offset:20992\n" \
"v_mfma_f32_16x16x32_bf16 v[20:23], a[64:67], v[228:231], v[20:23]\n" \
"v_mfma_f32_16x16x32_bf16 v[24:27], a[68:71], v[224:227], v[24:27]\n" \
"ds_read_b64_tr_b16 a[64:65], v11 offset:0\n" \
"ds_read_b64_tr_b16 a[66:67], v11 offset:512\n" \
"v_mfma_f32_16x16x32_bf16 v[28:31], a[68:71], v[228:231], v[28:31]\n" \
"s_add_u32 m0, s69, 53248\n" \
"v_add_u32 v4, 49152, v7\n" \
"v_mfma_f32_16x16x32_bf16 v[16:19], a[72:75], v[232:235], v[16:19]\n" \
"ds_read_b64_tr_b16 a[68:69], v0 offset:0\n" \
"ds_read_b64_tr_b16 a[70:71], v0 offset:512\n" \
"v_mfma_f32_16x16x32_bf16 v[20:23], a[72:75], v[236:239], v[20:23]\n" \
"buffer_load_dwordx4 v2, s[44:47], s59 offen lds\n" \
"s_add_u32 s59, s59, s66\n" \
"v_mfma_f32_16x16x32_bf16 v[24:27], a[76:79], v[232:235], v[24:27]\n" \
"ds_read_b64_tr_b16 a[72:73], v11 offset:4096\n" \
"ds_read_b64_tr_b16 a[74:75], v11 offset:4608\n" \
"v_mfma_f32_16x16x32_bf16 v[28:31], a[76:79], v[236:239], v[28:31]\n" \
"v_xor_b32 v5, 32, v4\n" \
"s_waitcnt lgkmcnt(6)\n" \
"v_mfma_f32_16x16x32_bf16 v[16:19], a[48:51], v[240:243], v[16:19]\n" \
"ds_read_b64_tr_b16 a[76:77], v0 offset:4096\n" \
"ds_read_b64_tr_b16 a[78:79], v0 offset:4608\n" \
"v_mfma_f32_16x16x32_bf16 v[20:23], a[48:51], v[244:247], v[20:23]\n" \
"v_mfma_f32_16x16x32_bf16 v[24:27], a[52:55], v[240:243], v[24:27]\n" \
"ds_read_b64_tr_b16 a[48:49], v11 offset:8192\n" \
"ds_read_b64_tr_b16 a[50:51], v11 offset:8704\n" \
"v_mfma_f32_16x16x32_bf16 v[28:31], a[52:55], v[244:247], v[28:31]\n" \
"s_add_u32 m0, s69, 57344\n" \
"v_mfma_f32_16x16x32_bf16 v[16:19], a[56:59], v[248:251], v[16:19]\n" \
"ds_read_b64_tr_b16 a[52:53], v0 offset:8192\n" \
"ds_read_b64_tr_b16 a[54:55], v0 offset:8704\n" \
"v_mfma_f32_16x16x32_bf16 v[20:23], a[56:59], v[252:255], v[20:23]\n" \
"buffer_load_dwordx4 v2, s[44:47], s59 offen lds\n" \
"s_add_u32 s59, s59, s66\n" \
"v_mfma_f32_16x16x32_bf16 v[24:27], a[60:63], v[248:251], v[24:27]\n" \
"ds_read_b64_tr_b16 a[56:57], v11 offset:12288\n" \
"ds_read_b64_tr_b16 a[58:59], v11 offset:12800\n" \
"v_mfma_f32_16x16x32_bf16 v[28:31], a[60:63], v[252:255], v[28:31]\n" \
"s_waitcnt lgkmcnt(6)\n" \
"v_mfma_f32_16x16x32_bf16 v[32:35], a[64:67], v[208:211], v[32:35]\n" \
"ds_read_b64_tr_b16 a[60:61], v0 offset:12288\n" \
"ds_read_b64_tr_b16 a[62:63], v0 offset:12800\n" \
"v_mfma_f32_16x16x32_bf16 v[36:39], a[64:67], v[212:215], v[36:39]\n" \
"s_add_u32 m0, s69, 61440\n" \
"v_mfma_f32_16x16x32_bf16 v[40:43], a[68:71], v[208:211], v[40:43]\n" \
"ds_read_b64_tr_b16 a[64:65], v11 offset:16384\n" \
"ds_read_b64_tr_b16 a[66:67], v11 offset:16896\n" \
"v_mfma_f32_16x16x32_bf16 v[44:47], a[68:71], v[212:215], v[44:47]\n" \
"buffer_load_dwordx4 v2, s[44:47], s59 offen lds\n" \
"s_add_u32 s59, s59, s66\n" \
"v_mfma_f32_16x16x32_bf16 v[32:35], a[72:75], v[216:219], v[32:35]\n" \
"ds_read_b64_tr_b16 a[68:69], v0 offset:16384\n" \
"ds_read_b64_tr_b16 a[70:71], v0 offset:16896\n" \
"v_mfma_f32_16x16x32_bf16 v[36:39], a[72:75], v[220:223], v[36:39]\n" \
"v_mfma_f32_16x16x32_bf16 v[40:43], a[76:79], v[216:219], v[40:43]\n" \
"ds_read_b64_tr_b16 a[72:73], v11 offset:20480\n" \
"ds_read_b64_tr_b16 a[74:75], v11 offset:20992\n" \
"v_mfma_f32_16x16x32_bf16 v[44:47], a[76:79], v[220:223], v[44:47]\n" \
"s_waitcnt lgkmcnt(6)\n" \
"v_mfma_f32_16x16x32_bf16 v[32:35], a[48:51], v[224:227], v[32:35]\n" \
"ds_read_b64_tr_b16 a[76:77], v0 offset:20480\n" \
"ds_read_b64_tr_b16 a[78:79], v0 offset:20992\n" \
"v_mfma_f32_16x16x32_bf16 v[36:39], a[48:51], v[228:231], v[36:39]\n" \
"s_waitcnt vmcnt(16)\n" \
"s_barrier\n" \
"v_mfma_f32_16x16x32_bf16 v[40:43], a[52:55], v[224:227], v[40:43]\n" \
"ds_read_b64_tr_b16 a[48:49], v4 offset:0\n" \
"ds_read_b64_tr_b16 a[50:51], v4 offset:512\n" \
"v_mfma_f32_16x16x32_bf16 v[44:47], a[52:55], v[228:231], v[44:47]\n" \
"v_xor_b32 v11, 64, v4\n" \
"v_xor_b32 v0, 96, v4\n" \
"v_mfma_f32_16x16x32_bf16 v[32:35], a[56:59], v[232:235], v[32:35]\n" \
"ds_read_b64_tr_b16 a[52:53], v5 offset:0\n" \
"ds_read_b64_tr_b16 a[54:55], v5 offset:512\n" \
"v_mfma_f32_16x16x32_bf16 v[36:39], a[56:59], v[236:239], v[36:39]\n" \
"s_add_u32 m0, s69, 65536\n" \
"s_xor_b32 s71, s71, 73728\n" \
"v_mfma_f32_16x16x32_bf16 v[40:43], a[60:63], v[232:235], v[40:43]\n" \
"ds_read_b64_tr_b16 a[56:57], v4 offset:4096\n" \
"ds_read_b64_tr_b16 a[58:59], v4 offset:4608\n" \
"v_mfma_f32_16x16x32_bf16 v[44:47], a[60:63], v[236:239], v[44:47]\n" \
"buffer_load_dwordx4 v2, s[44:47], s59 offen lds\n" \
"s_add_u32 s59, s59, s66\n" \
"s_waitcnt lgkmcnt(6)\n" \
"v_mfma_f32_16x16x32_bf16 v[32:35], a[64:67], v[240:243], v[32:35]\n" \
"ds_read_b64_tr_b16 a[60:61], v5 offset:4096\n" \
"ds_read_b64_tr_b16 a[62:63], v5 offset:4608\n" \
"v_mfma_f32_16x16x32_bf16 v[36:39], a[64:67], v[244:247], v[36:39]\n" \
"v_mfma_f32_16x16x32_bf16 v[40:43], a[68:71], v[240:243], v[40:43]\n" \
"ds_read_b64_tr_b16 a[64:65], v4 offset:8192\n" \
"ds_read_b64_tr_b16 a[66:67], v4 offset:8704\n" \
"v_mfma_f32_16x16x32_bf16 v[44:47], a[68:71], v[244:247], v[44:47]\n" \
"s_add_u32 m0, s69, 69632\n" \
"v_mfma_f32_16x16x32_bf16 v[32:35], a[72:75], v[248:251], v[32:35]\n" \
"ds_read_b64_tr_b16 a[68:69], v5 offset:8192\n" \
"ds_read_b64_tr_b16 a[70:71], v5 offset:8704\n" \
"v_mfma_f32_16x16x32_bf16 v[36:39], a[72:75], v[252:255], v[36:39]\n" \
"buffer_load_dwordx4 v2, s[44:47], s59 offen lds\n" \
"s_add_u32 s59, s59, s66\n" \
"v_mfma_f32_16x16x32_bf16 v[40:43], a[76:79], v[248:251], v[40:43]\n" \
"ds_read_b64_tr_b16 a[72:73], v4 offset:12288\n" \
"ds_read_b64_tr_b16 a[74:75], v4 offset:12800\n" \
"v_mfma_f32_16x16x32_bf16 v[44:47], a[76:79], v[252:255], v[44:47]\n" \
"s_cmp_eq_u32 s72, 4\n" \
"s_cbranch_scc1 Lsw_%=\n" \
"Lsb_%=:\n" \
"s_waitcnt lgkmcnt(6)\n" \
"v_mfma_f32_16x16x32_bf16 v[16:19], a[48:51], a[0:3], v[16:19]\n" \
"ds_read_b64_tr_b16 a[76:77], v5 offset:12288\n" \
"ds_read_b64_tr_b16 a[78:79], v5 offset:12800\n" \
"v_mfma_f32_16x16x32_bf16 v[20:23], a[48:51], a[4:7], v[20:23]\n" \
"s_add_u32 s59, s59, s68\n" \
"v_mfma_f32_16x16x32_bf16 v[24:27], a[52:55], a[0:3], v[24:27]\n" \
"ds_read_b64_tr_b16 a[48:49], v4 offset:16384\n" \
"ds_read_b64_tr_b16 a[50:51], v4 offset:16896\n" \
"v_mfma_f32_16x16x32_bf16 v[28:31], a[52:55], a[4:7], v[28:31]\n" \
"s_add_u32 m0, s70, 0\n" \
"v_mfma_f32_16x16x32_bf16 v[16:19], a[56:59], a[8:11], v[16:19]\n" \
"ds_read_b64_tr_b16 a[52:53], v5 offset:16384\n" \
"ds_read_b64_tr_b16 a[54:55], v5 offset:16896\n" \
"v_mfma_f32_16x16x32_bf16 v[20:23], a[56:59], a[12:15], v[20:23]\n" \
"buffer_load_dwordx4 v2, s[44:47], s59 offen lds\n" \
"s_add_u32 s59, s59, s66\n" \
"v_mfma_f32_16x16x32_bf16 v[24:27], a[60:63], a[8:11], v[24:27]\n" \
"ds_read_b64_tr_b16 a[56:57], v4 offset:20480\n" \
"ds_read_b64_tr_b16 a[58:59], v4 offset:20992\n" \
"v_mfma_f32_16x16x32_bf16 v[28:31], a[60:63], a[12:15], v[28:31]\n" \
"s_waitcnt lgkmcnt(6)\n" \
"v_mfma_f32_16x16x32_bf16 v[16:19], a[64:67], a[16:19], v[16:19]\n" \
"ds_read_b64_tr_b16 a[60:61], v5 offset:20480\n" \
"ds_read_b64_tr_b16 a[62:63], v5 offset:20992\n" \
"v_mfma_f32_16x16x32_bf16 v[20:23], a[64:67], a[20:23], v[20:23]\n" \
"v_mfma_f32_16x16x32_bf16 v[24:27], a[68:71], a[16:19], v[24:27]\n" \
"ds_read_b64_tr_b16 a[64:65], v11 offset:0\n" \
"ds_read_b64_tr_b16 a[66:67], v11 offset:512\n" \
"v_mfma_f32_16x16x32_bf16 v[28:31], a[68:71], a[20:23], v[28:31]\n" \
"s_add_u32 m0, s70, 4096\n" \
"v_add_u32 v4, s71, v6\n" \
"v_mfma_f32_16x16x32_bf16 v[16:19], a[72:75], a[24:27], v[16:19]\n" \
"ds_read_b64_tr_b16 a[68:69], v0 offset:0\n" \
"ds_read_b64_tr_b16 a[70:71], v0 offset:512\n" \
"v_mfma_f32_16x16x32_bf16 v[20:23], a[72:75], a[28:31], v[20:23]\n" \
"buffer_load_dwordx4 v2, s[44:47], s59 offen lds\n" \
"s_add_u32 s59, s59, s66\n" \
"v_mfma_f32_16x16x32_bf16 v[24:27], a[76:79], a[24:27], v[24:27]\n" \
"ds_read_b64_tr_b16 a[72:73], v11 offset:4096\n" \
"ds_read_b64_tr_b16 a[74:75], v11 offset:4608\n" \
"v_mfma_f32_16x16x32_bf16 v[28:31], a[76:79], a[28:31], v[28:31]\n" \
"v_xor_b32 v5, 32, v4\n" \
"s_waitcnt lgkmcnt(6)\n" \
"v_mfma_f32_16x16x32_bf16 v[16:19], a[48:51], a[32:35], v[16:19]\n" \
"ds_read_b64_tr_b16 a[76:77], v0 offset:4096\n" \
"ds_read_b64_tr_b16 a[78:79], v0 offset:4608\n" \
"v_mfma_f32_16x16x32_bf16 v[20:23], a[48:51], a[36:39], v[20:23]\n" \
"v_mov_b32 v7, v4\n" \
"v_mfma_f32_16x16x32_bf16 v[24:27], a[52:55], a[32:35], v[24:27]\n" \
"ds_read_b64_tr_b16 a[48:49], v11 offset:8192\n" \
"ds_read_b64_tr_b16 a[50:51], v11 offset:8704\n" \
"v_mfma_f32_16x16x32_bf16 v[28:31], a[52:55], a[36:39], v[28:31]\n" \
"s_add_u32 m0, s70, 8192\n" \
"v_mfma_f32_16x16x32_bf16 v[16:19], a[56:59], a[40:43], v[16:19]\n" \
"ds_read_b64_tr_b16 a[52:53], v0 offset:8192\n" \
"ds_read_b64_tr_b16 a[54:55], v0 offset:8704\n" \
"v_mfma_f32_16x16x32_bf16 v[20:23], a[56:59], a[44:47], v[20:23]\n" \
"buffer_load_dwordx4 v2, s[44:47], s59 offen lds\n" \
"s_add_u32 s59, s59, s66\n" \
"v_mfma_f32_16x16x32_bf16 v[24:27], a[60:63], a[40:43], v[24:27]\n" \
"ds_read_b64_tr_b16 a[56:57], v11 offset:12288\n" \
"ds_read_b64_tr_b16 a[58:59], v11 offset:12800\n" \
"v_mfma_f32_16x16x32_bf16 v[28:31], a[60:63], a[44:47], v[28:31]\n" \
"s_waitcnt lgkmcnt(6)\n" \
"v_mfma_f32_16x16x32_bf16 v[32:35], a[64:67], a[0:3], v[32:35]\n" \
"ds_read_b64_tr_b16 a[60:61], v0 offset:12288\n" \
"ds_read_b64_tr_b16 a[62:63], v0 offset:12800\n" \
"v_mfma_f32_16x16x32_bf16 v[36:39], a[64:67], a[4:7], v[36:39]\n" \
"s_add_u32 m0, s70, 12288\n" \
"v_mfma_f32_16x16x32_bf16 v[40:43], a[68:71], a[0:3], v[40:43]\n" \
"ds_read_b64_tr_b16 a[64:65], v11 offset:16384\n" \
"ds_read_b64_tr_b16 a[66:67], v11 offset:16896\n" \
"v_mfma_f32_16x16x32_bf16 v[44:47], a[68:71], a[4:7], v[44:47]\n" \
"buffer_load_dwordx4 v2, s[44:47], s59 offen lds\n" \
"s_add_u32 s59, s59, s66\n" \
"v_mfma_f32_16x16x32_bf16 v[32:35], a[72:75], a[8:11], v[32:35]\n" \
"ds_read_b64_tr_b16 a[68:69], v0 offset:16384\n" \
"ds_read_b64_tr_b16 a[70:71], v0 offset:16896\n" \
"v_mfma_f32_16x16x32_bf16 v[36:39], a[72:75], a[12:15], v[36:39]\n" \
"v_cvt_pk_bf16_f32 v48, v16, v17\n" \
"v_cvt_pk_bf16_f32 v49, v18, v19\n" \
"v_mfma_f32_16x16x32_bf16 v[40:43], a[76:79], a[8:11], v[40:43]\n" \
"ds_read_b64_tr_b16 a[72:73], v11 offset:20480\n" \
"ds_read_b64_tr_b16 a[74:75], v11 offset:20992\n" \
"v_mfma_f32_16x16x32_bf16 v[44:47], a[76:79], a[12:15], v[44:47]\n" \
"v_cvt_pk_bf16_f32 v50, v24, v25\n" \
"s_waitcnt lgkmcnt(6)\n" \
"v_mfma_f32_16x16x32_bf16 v[32:35], a[48:51], a[16:19], v[32:35]\n" \
"ds_read_b64_tr_b16 a[76:77], v0 offset:20480\n" \
"ds_read_b64_tr_b16 a[78:79], v0 offset:20992\n" \
"v_mfma_f32_16x16x32_bf16 v[36:39], a[48:51], a[20:23], v[36:39]\n" \
"s_waitcnt vmcnt(16)\n" \
"s_barrier\n" \
"v_mfma_f32_16x16x32_bf16 v[40:43], a[52:55], a[16:19], v[40:43]\n" \
"ds_read_b64_tr_b16 a[48:49], v4 offset:0\n" \
"ds_read_b64_tr_b16 a[50:51], v4 offset:512\n" \
"v_mfma_f32_16x16x32_bf16 v[44:47], a[52:55], a[20:23], v[44:47]\n" \
"v_cvt_pk_bf16_f32 v51, v26, v27\n" \
"v_cvt_pk_bf16_f32 v52, v20, v21\n" \
"v_mfma_f32_16x16x32_bf16 v[32:35], a[56:59], a[24:27], v[32:35]\n" \
"ds_read_b64_tr_b16 a[52:53], v5 offset:0\n" \
"ds_read_b64_tr_b16 a[54:55], v5 offset:512\n" \
"v_mfma_f32_16x16x32_bf16 v[36:39], a[56:59], a[28:31], v[36:39]\n" \
"s_add_u32 m0, s70, 16384\n" \
"v_cvt_pk_bf16_f32 v53, v22, v23\n" \
"v_mfma_f32_16x16x32_bf16 v[40:43], a[60:63], a[24:27], v[40:43]\n" \
"ds_read_b64_tr_b16 a[56:57], v4 offset:4096\n" \
"ds_read_b64_tr_b16 a[58:59], v4 offset:4608\n" \
"v_mfma_f32_16x16x32_bf16 v[44:47], a[60:63], a[28:31], v[44:47]\n" \
"buffer_load_dwordx4 v2, s[44:47], s59 offen lds\n" \
"s_add_u32 s59, s59, s66\n" \
"s_waitcnt lgkmcnt(6)\n" \
"v_mfma_f32_16x16x32_bf16 v[32:35], a[64:67], a[32:35], v[32:35]\n" \
"ds_read_b64_tr_b16 a[60:61], v5 offset:4096\n" \
"ds_read_b64_tr_b16 a[62:63], v5 offset:4608\n" \
"v_mfma_f32_16x16x32_bf16 v[36:39], a[64:67], a[36:39], v[36:39]\n" \
"v_cvt_pk_bf16_f32 v54, v28, v29\n" \
"v_cvt_pk_bf16_f32 v55, v30, v31\n" \
"v_mfma_f32_16x16x32_bf16 v[40:43], a[68:71], a[32:35], v[40:43]\n" \
"ds_read_b64_tr_b16 a[64:65], v4 offset:8192\n" \
"ds_read_b64_tr_b16 a[66:67], v4 offset:8704\n" \
"v_mfma_f32_16x16x32_bf16 v[44:47], a[68:71], a[36:39], v[44:47]\n" \
"s_add_u32 m0, s70, 20480\n" \
"v_mfma_f32_16x16x32_bf16 v[32:35], a[72:75], a[40:43], v[32:35]\n" \
"ds_read_b64_tr_b16 a[68:69], v5 offset:8192\n" \
"ds_read_b64_tr_b16 a[70:71], v5 offset:8704\n" \
"v_mfma_f32_16x16x32_bf16 v[36:39], a[72:75], a[44:47], v[36:39]\n" \
"buffer_load_dwordx4 v2, s[44:47], s59 offen lds\n" \
"s_add_u32 s59, s59, s66\n" \
"v_mfma_f32_16x16x32_bf16 v[40:43], a[76:79], a[40:43], v[40:43]\n" \
"ds_read_b64_tr_b16 a[72:73], v4 offset:12288\n" \
"ds_read_b64_tr_b16 a[74:75], v4 offset:12800\n" \
"s_mov_b32 s60, s69\n" \
"s_mov_b32 s69, s70\n" \
"v_mfma_f32_16x16x32_bf16 v[44:47], a[76:79], a[44:47], v[44:47]\n" \
"s_mov_b32 s70, s60\n" \
"s_add_u32 s72, s72, 1\n" \
"s_cmp_lt_u32 s72, 6\n" \
"s_cbranch_scc1 L1_%=\n" \
"s_mov_b32 s60, 0\n" \
"s_mov_b32 s75, 0x7f000000\n" \
"s_mov_b32 s72, 0\n" \
"s_mov_b32 s76, 0\n" \
"s_add_u32 s69, s58, 98304\n" \
"v_pk_max_i16 v144, v48, 0\n" \
"v_pk_max_i16 v145, v49, 0\n" \
"v_pk_max_i16 v146, v50, 0\n" \
"v_pk_max_i16 v147, v51, 0\n" \
"v_pk_max_i16 v148, v52, 0\n" \
"v_pk_max_i16 v149, v53, 0\n" \
"v_pk_max_i16 v150, v54, 0\n" \
"v_pk_max_i16 v151, v55, 0\n" \
"v_cvt_pk_bf16_f32 v56, v32, v33\n" \
"v_cvt_pk_bf16_f32 v57, v34, v35\n" \
"v_cvt_pk_bf16_f32 v58, v40, v41\n" \
"v_cvt_pk_bf16_f32 v59, v42, v43\n" \
"v_cvt_pk_bf16_f32 v60, v36, v37\n" \
"v_cvt_pk_bf16_f32 v61, v38, v39\n" \
"v_cvt_pk_bf16_f32 v62, v44, v45\n" \
"v_cvt_pk_bf16_f32 v63, v46, v47\n" \
"v_pk_max_i16 v152, v56, 0\n" \
"v_pk_max_i16 v153, v57, 0\n" \
"v_pk_max_i16 v154, v58, 0\n" \
"v_pk_max_i16 v155, v59, 0\n" \
"v_pk_max_i16 v156, v60, 0\n" \
"v_pk_max_i16 v157, v61, 0\n" \
"v_pk_max_i16 v158, v62, 0\n" \
"v_pk_max_i16 v159, v63, 0\n" \
"v_lshrrev_b32 v210, 4, v8\n" \
"v_min_u32 v208, 11, v210\n" \
"v_lshlrev_b32 v208, 11, v208\n" \
"s_lshl_b32 s92, s91, 11\n" \
"s_sub_u32 s92, s86, s92\n" \
"s_add_u32 s92, s92, 896\n" \
"v_add_u32 v208, s92, v208\n" \
"s_cmp_eq_u32 s61, 3\n" \
"s_cselect_b32 s94, 8, 99\n" \
"v_lshlrev_b32 v210, 2, v210\n" \
"v_add_u32 v210, 384, v210\n" \
"v_mov_b32 v211, 0\n" \
"v_mov_b32 v209, v13\n" \
"s_add_u32 s40, s84, s86\n" \
"s_addc_u32 s41, s85, 0\n" \
"s_and_b32 s41, s41, 0xffff\n" \
"s_cmp_eq_u32 s61, 0\n" \
"s_cselect_b32 s42, 1152, 0\n" \
"s_mov_b32 s62, 0\n" \
"v_mov_b32 v1, 0\n" \
"L2_%=:\n" \
"s_waitcnt lgkmcnt(6)\n" \
"v_mfma_f32_16x16x32_bf16 v[16:19], a[48:51], v[64:67], 0\n" \
"ds_read_b64_tr_b16 a[76:77], v5 offset:12288\n" \
"s_add_u32 s59, s59, s67\n" \
"v_mfma_f32_16x16x32_bf16 v[20:23], a[48:51], v[68:71], 0\n" \
"ds_read_b64_tr_b16 a[78:79], v5 offset:12800\n" \
"v_xor_b32 v11, 64, v4\n" \
"v_xor_b32 v0, 96, v4\n" \
"v_mfma_f32_16x16x32_bf16 v[24:27], a[52:55], v[64:67], 0\n" \
"ds_read_b64_tr_b16 a[48:49], v4 offset:16384\n" \
"s_add_u32 m0, s69, 0\n" \
"v_mfma_f32_16x16x32_bf16 v[28:31], a[52:55], v[68:71], 0\n" \
"ds_read_b64_tr_b16 a[50:51], v4 offset:16896\n" \
"buffer_load_dwordx4 v2, s[44:47], s59 offen lds\n" \
"v_mfma_f32_16x16x32_bf16 v[16:19], a[56:59], v[72:75], v[16:19]\n" \
"ds_read_b64_tr_b16 a[52:53], v5 offset:16384\n" \
"s_add_u32 s59, s59, s66\n" \
"v_mfma_f32_16x16x32_bf16 v[20:23], a[56:59], v[76:79], v[20:23]\n" \
"ds_read_b64_tr_b16 a[54:55], v5 offset:16896\n" \
"v_cvt_pk_bf16_f32 v168, v32, v33\n" \
"v_mfma_f32_16x16x32_bf16 v[24:27], a[60:63], v[72:75], v[24:27]\n" \
"ds_read_b64_tr_b16 a[56:57], v4 offset:20480\n" \
"v_cvt_pk_bf16_f32 v169, v34, v35\n" \
"v_mfma_f32_16x16x32_bf16 v[28:31], a[60:63], v[76:79], v[28:31]\n" \
"ds_read_b64_tr_b16 a[58:59], v4 offset:20992\n" \
"s_waitcnt lgkmcnt(6)\n" \
"v_mfma_f32_16x16x32_bf16 v[16:19], a[64:67], v[80:83], v[16:19]\n" \
"ds_read_b64_tr_b16 a[60:61], v5 offset:20480\n" \
"v_cvt_pk_bf16_f32 v170, v40, v41\n" \
"v_mfma_f32_16x16x32_bf16 v[20:23], a[64:67], v[84:87], v[20:23]\n" \
"ds_read_b64_tr_b16 a[62:63], v5 offset:20992\n" \
"v_cvt_pk_bf16_f32 v171, v42, v43\n" \
"v_mfma_f32_16x16x32_bf16 v[24:27], a[68:71], v[80:83], v[24:27]\n" \
"ds_read_b64_tr_b16 a[64:65], v11 offset:0\n" \
"s_add_u32 m0, s69, 4096\n" \
"v_mfma_f32_16x16x32_bf16 v[28:31], a[68:71], v[84:87], v[28:31]\n" \
"ds_read_b64_tr_b16 a[66:67], v11 offset:512\n" \
"buffer_load_dwordx4 v2, s[44:47], s59 offen lds\n" \
"v_mfma_f32_16x16x32_bf16 v[16:19], a[72:75], v[88:91], v[16:19]\n" \
"ds_read_b64_tr_b16 a[68:69], v0 offset:0\n" \
"s_add_u32 s59, s59, s66\n" \
"v_mfma_f32_16x16x32_bf16 v[20:23], a[72:75], v[92:95], v[20:23]\n" \
"ds_read_b64_tr_b16 a[70:71], v0 offset:512\n" \
"v_cvt_pk_bf16_f32 v172, v36, v37\n" \
"v_mfma_f32_16x16x32_bf16 v[24:27], a[76:79], v[88:91], v[24:27]\n" \
"ds_read_b64_tr_b16 a[72:73], v11 offset:4096\n" \
"v_cvt_pk_bf16_f32 v173, v38, v39\n" \
"v_mfma_f32_16x16x32_bf16 v[28:31], a[76:79], v[92:95], v[28:31]\n" \
"ds_read_b64_tr_b16 a[74:75], v11 offset:4608\n" \
"s_waitcnt lgkmcnt(6)\n" \
"v_mfma_f32_16x16x32_bf16 v[16:19], a[48:51], v[96:99], v[16:19]\n" \
"ds_read_b64_tr_b16 a[76:77], v0 offset:4096\n" \
"v_cvt_pk_bf16_f32 v174, v44, v45\n" \
"v_mfma_f32_16x16x32_bf16 v[20:23], a[48:51], v[100:103], v[20:23]\n" \
"ds_read_b64_tr_b16 a[78:79], v0 offset:4608\n" \
"v_cvt_pk_bf16_f32 v175, v46, v47\n" \
"v_mfma_f32_16x16x32_bf16 v[24:27], a[52:55], v[96:99], v[24:27]\n" \
"ds_read_b64_tr_b16 a[48:49], v11 offset:8192\n" \
"s_add_u32 m0, s69, 8192\n" \
"v_mfma_f32_16x16x32_bf16 v[28:31], a[52:55], v[100:103], v[28:31]\n" \
"ds_read_b64_tr_b16 a[50:51], v11 offset:8704\n" \
"buffer_load_dwordx4 v2, s[44:47], s59 offen lds\n" \
"v_mfma_f32_16x16x32_bf16 v[16:19], a[56:59], v[104:107], v[16:19]\n" \
"ds_read_b64_tr_b16 a[52:53], v0 offset:8192\n" \
"s_add_u32 s59, s59, s66\n" \
"v_mfma_f32_16x16x32_bf16 v[20:23], a[56:59], v[108:111], v[20:23]\n" \
"ds_read_b64_tr_b16 a[54:55], v0 offset:8704\n" \
"v_add_u32 v4, 24576, v4\n" \
"v_mfma_f32_16x16x32_bf16 v[24:27], a[60:63], v[104:107], v[24:27]\n" \
"ds_read_b64_tr_b16 a[56:57], v11 offset:12288\n" \
"v_xor_b32 v5, 32, v4\n" \
"v_mfma_f32_16x16x32_bf16 v[28:31], a[60:63], v[108:111], v[28:31]\n" \
"ds_read_b64_tr_b16 a[58:59], v11 offset:12800\n" \
"s_waitcnt lgkmcnt(6)\n" \
"v_mfma_f32_16x16x32_bf16 v[32:35], a[64:67], v[64:67], 0\n" \
"ds_read_b64_tr_b16 a[60:61], v0 offset:12288\n" \
"buffer_store_dwordx4 v[168:171], v8, s[52:55], s75 offen sc1\n" \
"v_mfma_f32_16x16x32_bf16 v[36:39], a[64:67], v[68:71], 0\n" \
"ds_read_b64_tr_b16 a[62:63], v0 offset:12800\n" \
"s_add_u32 m0, s69, 12288\n" \
"v_mfma_f32_16x16x32_bf16 v[40:43], a[68:71], v[64:67], 0\n" \
"ds_read_b64_tr_b16 a[64:65], v11 offset:16384\n" \
"buffer_load_dwordx4 v2, s[44:47], s59 offen lds\n" \
"v_mfma_f32_16x16x32_bf16 v[44:47], a[68:71], v[68:71], 0\n" \
"ds_read_b64_tr_b16 a[66:67], v11 offset:16896\n" \
"s_add_u32 s59, s59, s66\n" \
"v_mfma_f32_16x16x32_bf16 v[32:35], a[72:75], v[72:75], v[32:35]\n" \
"ds_read_b64_tr_b16 a[68:69], v0 offset:16384\n" \
"v_mfma_f32_16x16x32_bf16 v[36:39], a[72:75], v[76:79], v[36:39]\n" \
"ds_read_b64_tr_b16 a[70:71], v0 offset:16896\n" \
"v_mfma_f32_16x16x32_bf16 v[40:43], a[76:79], v[72:75], v[40:43]\n" \
"ds_read_b64_tr_b16 a[72:73], v11 offset:20480\n" \
"buffer_store_dwordx4 v[172:175], v8, s[52:55], s75 offen offset:1024 sc1\n" \
"v_mfma_f32_16x16x32_bf16 v[44:47], a[76:79], v[76:79], v[44:47]\n" \
"ds_read_b64_tr_b16 a[74:75], v11 offset:20992\n" \
"s_waitcnt lgkmcnt(6)\n" \
"v_mfma_f32_16x16x32_bf16 v[32:35], a[48:51], v[80:83], v[32:35]\n" \
"ds_read_b64_tr_b16 a[76:77], v0 offset:20480\n" \
"s_add_u32 s75, s60, 98304\n" \
"v_mfma_f32_16x16x32_bf16 v[36:39], a[48:51], v[84:87], v[36:39]\n" \
"ds_read_b64_tr_b16 a[78:79], v0 offset:20992\n" \
"s_waitcnt vmcnt(16)\n" \
"s_barrier\n" \
"v_mfma_f32_16x16x32_bf16 v[40:43], a[52:55], v[80:83], v[40:43]\n" \
"ds_read_b64_tr_b16 a[48:49], v4 offset:0\n" \
"buffer_store_dword v13, v1, s[40:43], s62 offen sc1\n" \
"v_mfma_f32_16x16x32_bf16 v[44:47], a[52:55], v[84:87], v[44:47]\n" \
"ds_read_b64_tr_b16 a[50:51], v4 offset:512\n" \
"v_xor_b32 v11, 64, v4\n" \
"v_mfma_f32_16x16x32_bf16 v[32:35], a[56:59], v[88:91], v[32:35]\n" \
"ds_read_b64_tr_b16 a[52:53], v5 offset:0\n" \
"s_cmp_eq_u32 s72, s94\n" \
"s_cbranch_scc1 Lpf_%=\n" \
"Lpb_%=:\n" \
"v_mfma_f32_16x16x32_bf16 v[36:39], a[56:59], v[92:95], v[36:39]\n" \
"ds_read_b64_tr_b16 a[54:55], v5 offset:512\n" \
"s_add_u32 m0, s69, 16384\n" \
"v_mfma_f32_16x16x32_bf16 v[40:43], a[60:63], v[88:91], v[40:43]\n" \
"ds_read_b64_tr_b16 a[56:57], v4 offset:4096\n" \
"buffer_load_dwordx4 v2, s[44:47], s59 offen lds\n" \
"v_mfma_f32_16x16x32_bf16 v[44:47], a[60:63], v[92:95], v[44:47]\n" \
"ds_read_b64_tr_b16 a[58:59], v4 offset:4608\n" \
"s_waitcnt lgkmcnt(6)\n" \
"v_mfma_f32_16x16x32_bf16 v[32:35], a[64:67], v[96:99], v[32:35]\n" \
"ds_read_b64_tr_b16 a[60:61], v5 offset:4096\n" \
"s_add_u32 s59, s59, s66\n" \
"v_mfma_f32_16x16x32_bf16 v[36:39], a[64:67], v[100:103], v[36:39]\n" \
"ds_read_b64_tr_b16 a[62:63], v5 offset:4608\n" \
"s_add_u32 s76, s76, 49152\n" \
"v_mfma_f32_16x16x32_bf16 v[40:43], a[68:71], v[96:99], v[40:43]\n" \
"ds_read_b64_tr_b16 a[64:65], v4 offset:8192\n" \
"s_add_u32 m0, s69, 20480\n" \
"v_mfma_f32_16x16x32_bf16 v[44:47], a[68:71], v[100:103], v[44:47]\n" \
"ds_read_b64_tr_b16 a[66:67], v4 offset:8704\n" \
"buffer_load_dwordx4 v2, s[44:47], s59 offen lds\n" \
"v_mfma_f32_16x16x32_bf16 v[32:35], a[72:75], v[104:107], v[32:35]\n" \
"ds_read_b64_tr_b16 a[68:69], v5 offset:8192\n" \
"s_add_u32 s59, s59, s66\n" \
"v_mfma_f32_16x16x32_bf16 v[36:39], a[72:75], v[108:111], v[36:39]\n" \
"ds_read_b64_tr_b16 a[70:71], v5 offset:8704\n" \
"s_cmp_eq_u32 s76, 147456\n" \
"s_cselect_b32 s76, 0, s76\n" \
"v_mfma_f32_16x16x32_bf16 v[40:43], a[76:79], v[104:107], v[40:43]\n" \
"ds_read_b64_tr_b16 a[72:73], v4 offset:12288\n" \
"v_xor_b32 v0, 96, v4\n" \
"v_mfma_f32_16x16x32_bf16 v[44:47], a[76:79], v[108:111], v[44:47]\n" \
"ds_read_b64_tr_b16 a[74:75], v4 offset:12800\n" \
"s_waitcnt lgkmcnt(6)\n" \
"v_mfma_f32_16x16x32_bf16 v[16:19], a[48:51], v[112:115], v[16:19]\n" \
"ds_read_b64_tr_b16 a[76:77], v5 offset:12288\n" \
"s_add_u32 s62, s62, 128\n" \
"v_mfma_f32_16x16x32_bf16 v[20:23], a[48:51], v[116:119], v[20:23]\n" \
"ds_read_b64_tr_b16 a[78:79], v5 offset:12800\n" \
"s_add_u32 s72, s72, 1\n" \
"v_mfma_f32_16x16x32_bf16 v[24:27], a[52:55], v[112:115], v[24:27]\n" \
"ds_read_b64_tr_b16 a[48:49], v4 offset:16384\n" \
"s_add_u32 m0, s69, 24576\n" \
"v_mfma_f32_16x16x32_bf16 v[28:31], a[52:55], v[116:119], v[28:31]\n" \
"ds_read_b64_tr_b16 a[50:51], v4 offset:16896\n" \
"buffer_load_dwordx4 v2, s[44:47], s59 offen lds\n" \
"v_mfma_f32_16x16x32_bf16 v[16:19], a[56:59], v[120:123], v[16:19]\n" \
"ds_read_b64_tr_b16 a[52:53], v5 offset:16384\n" \
"s_add_u32 s59, s59, s66\n" \
"v_mfma_f32_16x16x32_bf16 v[20:23], a[56:59], v[124:127], v[20:23]\n" \
"ds_read_b64_tr_b16 a[54:55], v5 offset:16896\n" \
"s_add_u32 s71, s69, 49152\n" \
"v_mfma_f32_16x16x32_bf16 v[24:27], a[60:63], v[120:123], v[24:27]\n" \
"ds_read_b64_tr_b16 a[56:57], v4 offset:20480\n" \
"s_sub_u32 s63, s71, 147456\n" \
"v_mfma_f32_16x16x32_bf16 v[28:31], a[60:63], v[124:127], v[28:31]\n" \
"ds_read_b64_tr_b16 a[58:59], v4 offset:20992\n" \
"s_waitcnt lgkmcnt(6)\n" \
"v_mfma_f32_16x16x32_bf16 v[16:19], a[64:67], v[128:131], v[16:19]\n" \
"ds_read_b64_tr_b16 a[60:61], v5 offset:20480\n" \
"s_cmp_ge_u32 s71, 147456\n" \
"s_cselect_b32 s71, s63, s71\n" \
"v_mfma_f32_16x16x32_bf16 v[20:23], a[64:67], v[132:135], v[20:23]\n" \
"ds_read_b64_tr_b16 a[62:63], v5 offset:20992\n" \
"s_cmp_ge_u32 s72, 7\n" \
"s_cselect_b32 s64, 0, s46\n" \
"v_mfma_f32_16x16x32_bf16 v[24:27], a[68:71], v[128:131], v[24:27]\n" \
"ds_read_b64_tr_b16 a[64:65], v11 offset:0\n" \
"s_add_u32 m0, s69, 28672\n" \
"v_mfma_f32_16x16x32_bf16 v[28:31], a[68:71], v[132:135], v[28:31]\n" \
"ds_read_b64_tr_b16 a[66:67], v11 offset:512\n" \
"buffer_load_dwordx4 v2, s[44:47], s59 offen lds\n" \
"v_mfma_f32_16x16x32_bf16 v[16:19], a[72:75], v[136:139], v[16:19]\n" \
"ds_read_b64_tr_b16 a[68:69], v0 offset:0\n" \
"s_add_u32 s59, s59, s66\n" \
"v_mfma_f32_16x16x32_bf16 v[20:23], a[72:75], v[140:143], v[20:23]\n" \
"ds_read_b64_tr_b16 a[70:71], v0 offset:512\n" \
"v_add_u32 v4, s76, v6\n" \
"v_mfma_f32_16x16x32_bf16 v[24:27], a[76:79], v[136:139], v[24:27]\n" \
"ds_read_b64_tr_b16 a[72:73], v11 offset:4096\n" \
"v_xor_b32 v5, 32, v4\n" \
"v_mfma_f32_16x16x32_bf16 v[28:31], a[76:79], v[140:143], v[28:31]\n" \
"ds_read_b64_tr_b16 a[74:75], v11 offset:4608\n" \
"s_waitcnt lgkmcnt(6)\n" \
"v_mfma_f32_16x16x32_bf16 v[16:19], a[48:51], v[144:147], v[16:19]\n" \
"ds_read_b64_tr_b16 a[76:77], v0 offset:4096\n" \
"v_mfma_f32_16x16x32_bf16 v[20:23], a[48:51], v[148:151], v[20:23]\n" \
"ds_read_b64_tr_b16 a[78:79], v0 offset:4608\n" \
"v_mfma_f32_16x16x32_bf16 v[24:27], a[52:55], v[144:147], v[24:27]\n" \
"ds_read_b64_tr_b16 a[48:49], v11 offset:8192\n" \
"s_add_u32 m0, s69, 32768\n" \
"v_mfma_f32_16x16x32_bf16 v[28:31], a[52:55], v[148:151], v[28:31]\n" \
"ds_read_b64_tr_b16 a[50:51], v11 offset:8704\n" \
"buffer_load_dwordx4 v2, s[44:47], s59 offen lds\n" \
"v_mfma_f32_16x16x32_bf16 v[16:19], a[56:59], v[152:155], v[16:19]\n" \
"ds_read_b64_tr_b16 a[52:53], v0 offset:8192\n" \
"s_add_u32 s59, s59, s66\n" \
"v_mfma_f32_16x16x32_bf16 v[20:23], a[56:59], v[156:159], v[20:23]\n" \
"ds_read_b64_tr_b16 a[54:55], v0 offset:8704\n" \
"v_mfma_f32_16x16x32_bf16 v[24:27], a[60:63], v[152:155], v[24:27]\n" \
"ds_read_b64_tr_b16 a[56:57], v11 offset:12288\n" \
"v_mfma_f32_16x16x32_bf16 v[28:31], a[60:63], v[156:159], v[28:31]\n" \
"ds_read_b64_tr_b16 a[58:59], v11 offset:12800\n" \
"s_waitcnt lgkmcnt(6)\n" \
"v_mfma_f32_16x16x32_bf16 v[32:35], a[64:67], v[112:115], v[32:35]\n" \
"ds_read_b64_tr_b16 a[60:61], v0 offset:12288\n" \
"s_add_u32 m0, s69, 36864\n" \
"v_mfma_f32_16x16x32_bf16 v[36:39], a[64:67], v[116:119], v[36:39]\n" \
"ds_read_b64_tr_b16 a[62:63], v0 offset:12800\n" \
"buffer_load_dwordx4 v2, s[44:47], s59 offen lds\n" \
"v_mfma_f32_16x16x32_bf16 v[40:43], a[68:71], v[112:115], v[40:43]\n" \
"ds_read_b64_tr_b16 a[64:65], v11 offset:16384\n" \
"s_add_u32 s59, s59, s66\n" \
"v_mfma_f32_16x16x32_bf16 v[44:47], a[68:71], v[116:119], v[44:47]\n" \
"ds_read_b64_tr_b16 a[66:67], v11 offset:16896\n" \
"v_cvt_pk_bf16_f32 v160, v16, v17\n" \
"v_mfma_f32_16x16x32_bf16 v[32:35], a[72:75], v[120:123], v[32:35]\n" \
"ds_read_b64_tr_b16 a[68:69], v0 offset:16384\n" \
"v_cvt_pk_bf16_f32 v161, v18, v19\n" \
"v_mfma_f32_16x16x32_bf16 v[36:39], a[72:75], v[124:127], v[36:39]\n" \
"ds_read_b64_tr_b16 a[70:71], v0 offset:16896\n" \
"v_cvt_pk_bf16_f32 v162, v24, v25\n" \
"v_mfma_f32_16x16x32_bf16 v[40:43], a[76:79], v[120:123], v[40:43]\n" \
"ds_read_b64_tr_b16 a[72:73], v11 offset:20480\n" \
"v_cvt_pk_bf16_f32 v163, v26, v27\n" \
"v_mfma_f32_16x16x32_bf16 v[44:47], a[76:79], v[124:127], v[44:47]\n" \
"ds_read_b64_tr_b16 a[74:75], v11 offset:20992\n" \
"s_waitcnt lgkmcnt(6)\n" \
"v_mfma_f32_16x16x32_bf16 v[32:35], a[48:51], v[128:131], v[32:35]\n" \
"ds_read_b64_tr_b16 a[76:77], v0 offset:20480\n" \
"v_cvt_pk_bf16_f32 v164, v20, v21\n" \
"v_mfma_f32_16x16x32_bf16 v[36:39], a[48:51], v[132:135], v[36:39]\n" \
"ds_read_b64_tr_b16 a[78:79], v0 offset:20992\n" \
"s_waitcnt vmcnt(16)\n" \
"s_barrier\n" \
"v_mfma_f32_16x16x32_bf16 v[40:43], a[52:55], v[128:131], v[40:43]\n" \
"ds_read_b64_tr_b16 a[48:49], v4 offset:0\n" \
"v_cvt_pk_bf16_f32 v165, v22, v23\n" \
"v_mfma_f32_16x16x32_bf16 v[44:47], a[52:55], v[132:135], v[44:47]\n" \
"ds_read_b64_tr_b16 a[50:51], v4 offset:512\n" \
"v_cvt_pk_bf16_f32 v166, v28, v29\n" \
"v_mfma_f32_16x16x32_bf16 v[32:35], a[56:59], v[136:139], v[32:35]\n" \
"ds_read_b64_tr_b16 a[52:53], v5 offset:0\n" \
"s_add_u32 m0, s69, 40960\n" \
"v_mfma_f32_16x16x32_bf16 v[36:39], a[56:59], v[140:143], v[36:39]\n" \
"ds_read_b64_tr_b16 a[54:55], v5 offset:512\n" \
"buffer_load_dwordx4 v2, s[44:47], s59 offen lds\n" \
"v_mfma_f32_16x16x32_bf16 v[40:43], a[60:63], v[136:139], v[40:43]\n" \
"ds_read_b64_tr_b16 a[56:57], v4 offset:4096\n" \
"s_add_u32 s59, s59, s66\n" \
"v_mfma_f32_16x16x32_bf16 v[44:47], a[60:63], v[140:143], v[44:47]\n" \
"ds_read_b64_tr_b16 a[58:59], v4 offset:4608\n" \
"s_waitcnt lgkmcnt(6)\n" \
"v_mfma_f32_16x16x32_bf16 v[32:35], a[64:67], v[144:147], v[32:35]\n" \
"ds_read_b64_tr_b16 a[60:61], v5 offset:4096\n" \
"v_cvt_pk_bf16_f32 v167, v30, v31\n" \
"v_mfma_f32_16x16x32_bf16 v[36:39], a[64:67], v[148:151], v[36:39]\n" \
"ds_read_b64_tr_b16 a[62:63], v5 offset:4608\n" \
"v_mfma_f32_16x16x32_bf16 v[40:43], a[68:71], v[144:147], v[40:43]\n" \
"ds_read_b64_tr_b16 a[64:65], v4 offset:8192\n" \
"buffer_store_dwordx4 v[160:163], v8, s[52:55], s60 offen sc1\n" \
"v_mfma_f32_16x16x32_bf16 v[44:47], a[68:71], v[148:151], v[44:47]\n" \
"ds_read_b64_tr_b16 a[66:67], v4 offset:8704\n" \
"s_add_u32 m0, s69, 45056\n" \
"v_mfma_f32_16x16x32_bf16 v[32:35], a[72:75], v[152:155], v[32:35]\n" \
"ds_read_b64_tr_b16 a[68:69], v5 offset:8192\n" \
"buffer_load_dwordx4 v2, s[44:47], s59 offen lds\n" \
"v_mfma_f32_16x16x32_bf16 v[36:39], a[72:75], v[156:159], v[36:39]\n" \
"ds_read_b64_tr_b16 a[70:71], v5 offset:8704\n" \
"s_add_u32 s59, s59, s66\n" \
"v_mfma_f32_16x16x32_bf16 v[40:43], a[76:79], v[152:155], v[40:43]\n" \
"ds_read_b64_tr_b16 a[72:73], v4 offset:12288\n" \
"buffer_store_dwordx4 v[164:167], v8, s[52:55], s60 offen offset:1024 sc1\n" \
"v_mfma_f32_16x16x32_bf16 v[44:47], a[76:79], v[156:159], v[44:47]\n" \
"ds_read_b64_tr_b16 a[74:75], v4 offset:12800\n" \
"s_mov_b32 s69, s71\n" \
"s_mov_b32 s46, s64\n" \
"s_add_u32 s60, s60, 196608\n" \
"s_cmp_lt_u32 s72, 9\n" \
"s_cbranch_scc1 L2_%=\n" \
"s_nop 7\n" \
"v_cvt_pk_bf16_f32 v168, v32, v33\n" \
"v_cvt_pk_bf16_f32 v169, v34, v35\n" \
"v_cvt_pk_bf16_f32 v170, v40, v41\n" \
"v_cvt_pk_bf16_f32 v171, v42, v43\n" \
"v_cvt_pk_bf16_f32 v172, v36, v37\n" \
"v_cvt_pk_bf16_f32 v173, v38, v39\n" \
"v_cvt_pk_bf16_f32 v174, v44, v45\n" \
"v_cvt_pk_bf16_f32 v175, v46, v47\n" \
"buffer_store_dwordx4 v[168:171], v8, s[52:55], s75 offen sc1\n" \
"buffer_store_dwordx4 v[172:175], v8, s[52:55], s75 offen offset:1024 sc1\n" \
"s_waitcnt vmcnt(0) lgkmcnt(0)\n" \
"v_cmp_eq_u32 vcc, s88, v209\n" \
"s_mov_b64 s[92:93], vcc\n" \
"v_cmp_eq_u32 vcc, s88, v210\n" \
"s_and_b64 vcc, vcc, s[92:93]\n" \
"s_cmp_eq_u64 vcc, -1\n" \
"s_cselect_b32 s95, 1, 0\n" \
"v_lshrrev_b32 v0, 4, v8\n" \
"s_branch Lfin_%=\n" \
"Lsw_%=:\n" \
"s_mov_b64 s[44:45], s[48:49]\n" \
"s_mov_b32 s46, 442368\n" \
"v_mov_b32 v2, v3\n" \
"s_mov_b32 s66, 36864\n" \
"s_mov_b32 s59, s57\n" \
"s_mov_b32 s67, 4294525056\n" \
"s_mov_b32 s68, 0\n" \
"s_branch Lsb_%=\n" \
"Lpf_%=:\n" \
"s_mov_b64 exec, 0xfff\n" \
"global_atomic_add v209, v208, v211, s[84:85] sc0 sc1\n" \
"s_mov_b64 exec, -1\n" \
"global_load_dword v210, v210, s[84:85] sc1\n" \
"s_branch Lpb_%=\n" \
"Lfin_%=:\n" \
""
constexpr int W_ST = 0, W_CP = 8448, W_KT = 25088, W_U = 26640, W_BYTES = 31264, SH_CP = 125056, SH_KT = 141696, SH_U = 143248, SH_EX = 147856, SH_FL = 156048;
#define SSM_BODY \
"v_mbcnt_lo_u32_b32 v0, -1, 0\n" \
"v_mbcnt_hi_u32_b32 v0, -1, v0\n" \
"s_mov_b32 s70, 0x3fb8aa3b\n" \
"s_mov_b32 s71, 0x3fb8aa3b\n" \
"s_mov_b32 s72, 0x3e22f983\n" \
"s_mov_b32 s73, 0x3e22f983\n" \
"s_mov_b32 s80, 0x41000000\n" \
"s_mov_b32 s81, 0x41000000\n" \
"s_mov_b32 s82, 0x43000000\n" \
"s_mov_b32 s83, 0x43000000\n" \
"s_mov_b32 s84, 0xc1000000\n" \
"s_mov_b32 s85, 0xc1000000\n" \
"s_mov_b32 s86, 0x05040100\n" \
"s_mov_b32 s66, 1\n" \
"s_mov_b32 s46, 2359296\n" \
"s_mov_b32 s47, 0x20000\n" \
"s_mov_b32 s50, 2359296\n" \
"s_mov_b32 s51, 0x20000\n" \
"s_mov_b32 s62, 41472\n" \
"s_mov_b32 s63, 0x20000\n" \
"s_and_b32 s45, s45, 0xffff\n" \
"s_and_b32 s49, s49, 0xffff\n" \
"s_and_b32 s61, s61, 0xffff\n" \
"s_and_b32 s93, s93, 0xffff\n" \
"s_mov_b32 s94, 165888\n" \
"s_mov_b32 s95, 0x20000\n" \
"buffer_load_dword a250, v76, s[92:95], 0 offen offset:256\n" \
"buffer_load_dword a250, v76, s[92:95], 0 offen offset:384\n" \
"v_mov_b32 v16, 0\n" \
"v_mov_b32 v17, 0\n" \
"v_mov_b32 v18, 0\n" \
"v_mov_b32 v19, 0\n" \
"v_add_u32 v3, s41, v78\n" \
"v_mov_b32 v4, v78\n" \
"s_mov_b32 exec_hi, 0\n" \
"ds_write_b128 v3, v[16:19] offset:26640\n" \
"v_add_u32 v4, 143248, v4\n" \
"ds_write_b128 v4, v[16:19]\n" \
"s_mov_b64 exec, -1\n" \
"s_cmp_lg_u32 s40, 0\n" \
"s_cbranch_scc1 Lk0_%=\n" \
"v_mov_b32 v240, v96\n" \
"v_mov_b32 v241, v97\n" \
"v_mov_b32 v242, v98\n" \
"v_mov_b32 v243, v99\n" \
"v_mov_b32 v244, v100\n" \
"v_mov_b32 v245, v101\n" \
"v_mov_b32 v246, v102\n" \
"v_mov_b32 v247, v103\n" \
"s_sub_u32 s88, 116608, s41\n" \
"s_getpc_b64 s[64:65]\n" \
"Lr1_%=:\n" \
"s_add_u32 s64, s64, Lb1_%=-Lr1_%=\n" \
"s_addc_u32 s65, s65, 0\n" \
"s_branch Lkg_%=\n" \
"Lb1_%=:\n" \
"s_waitcnt vmcnt(0)\n" \
"s_waitcnt lgkmcnt(0)\n" \
"v_mov_b32 v3, 156048\n" \
"v_mov_b32 v4, 1\n" \
"ds_write_b32 v3, v4\n" \
"Lk0_%=:\n" \
"v_mov_b32 v240, v80\n" \
"v_mov_b32 v241, v81\n" \
"v_mov_b32 v242, v82\n" \
"v_mov_b32 v243, v83\n" \
"v_mov_b32 v244, v84\n" \
"v_mov_b32 v245, v85\n" \
"v_mov_b32 v246, v86\n" \
"v_mov_b32 v247, v87\n" \
"s_mov_b32 s88, 0\n" \
"s_getpc_b64 s[64:65]\n" \
"Lr2_%=:\n" \
"s_add_u32 s64, s64, Lb2_%=-Lr2_%=\n" \
"s_addc_u32 s65, s65, 0\n" \
"s_branch Lkg_%=\n" \
"Lb2_%=:\n" \
"s_mov_b32 s68, 0x400000\n" \
"Lpl_%=:\n" \
"s_waitcnt vmcnt(0)\n" \
"v_cmp_eq_u32 vcc, s54, v4\n" \
"s_cmp_eq_u64 vcc, -1\n" \
"s_cbranch_scc1 Lpx_%=\n" \
"s_sleep 4\n" \
"global_load_dword v4, v3, s[52:53] sc1\n" \
"s_sub_u32 s68, s68, 1\n" \
"s_cbranch_scc0 Lpl_%=\n" \
"Lpx_%=:\n" \
"s_add_u32 s87, s55, s40\n" \
"s_lshl_b32 s67, s87, 6\n" \
"buffer_load_dwordx4 v[224:227], v75, s[44:47], s67 offen sc1\n" \
"s_add_u32 s67, s67, 589824\n" \
"buffer_load_dwordx4 v[228:231], v75, s[44:47], s67 offen sc1\n" \
"s_add_u32 s67, s67, 589824\n" \
"buffer_load_dwordx4 v[232:235], v75, s[44:47], s67 offen sc1\n" \
"s_add_u32 s67, s67, 589824\n" \
"buffer_load_dwordx4 v[236:239], v75, s[44:47], s67 offen sc1\n" \
"s_add_u32 s67, s67, 589824\n" \
"s_lshl_b32 s67, s87, 6\n" \
"buffer_load_dwordx4 v[240:243], v75, s[44:47], s67 offen offset:256 sc1\n" \
"s_add_u32 s67, s67, 589824\n" \
"buffer_load_dwordx4 v[244:247], v75, s[44:47], s67 offen offset:256 sc1\n" \
"s_add_u32 s67, s67, 589824\n" \
"buffer_load_dwordx4 v[248:251], v75, s[44:47], s67 offen offset:256 sc1\n" \
"s_add_u32 s67, s67, 589824\n" \
"buffer_load_dwordx4 v[252:255], v75, s[44:47], s67 offen offset:256 sc1\n" \
"s_add_u32 s67, s67, 589824\n" \
"s_add_u32 s67, s55, 8\n" \
"s_lshl_b32 s67, s67, 6\n" \
"buffer_load_dwordx4 a[128:131], v75, s[44:47], s67 offen sc1\n" \
"s_add_u32 s67, s67, 589824\n" \
"buffer_load_dwordx4 a[132:135], v75, s[44:47], s67 offen sc1\n" \
"s_add_u32 s67, s67, 589824\n" \
"buffer_load_dwordx4 a[136:139], v75, s[44:47], s67 offen sc1\n" \
"s_add_u32 s67, s67, 589824\n" \
"buffer_load_dwordx4 a[140:143], v75, s[44:47], s67 offen sc1\n" \
"s_add_u32 s67, s67, 589824\n" \
"buffer_load_dword a250, v76, s[92:95], 0 offen offset:512\n" \
"buffer_load_dword a250, v76, s[92:95], 0 offen offset:640\n" \
"s_mov_b32 s67, 0\n" \
"s_getpc_b64 s[64:65]\n" \
"Lr3_%=:\n" \
"s_add_u32 s64, s64, Lb3_%=-Lr3_%=\n" \
"s_addc_u32 s65, s65, 0\n" \
"s_branch Lex_%=\n" \
"Lb3_%=:\n" \
"s_waitcnt vmcnt(10)\n" \
"s_add_u32 s67, s41, 26640\n" \
"v_add_u32 v3, s67, v78\n" \
"ds_write_b128 v3, v[224:227] offset:512\n" \
"ds_write_b128 v3, v[228:231] offset:1536\n" \
"ds_write_b128 v3, v[232:235] offset:2560\n" \
"ds_write_b128 v3, v[236:239] offset:3584\n" \
"v_mov_b32 v1, v69\n" \
"v_mov_b32 v2, v70\n" \
"v_add_u32 v1, -384, v1\n" \
"v_add_u32 v2, -384, v2\n" \
"ds_read_b128 a[32:35], v2 offset:448\n" \
"ds_read_b64 a[0:1], v1 offset:4032\n" \
"ds_read_b64 a[2:3], v1 offset:4040\n" \
"ds_read_b128 a[36:39], v2 offset:960\n" \
"ds_read_b64 a[4:5], v1 offset:3520\n" \
"ds_read_b64 a[6:7], v1 offset:3528\n" \
"ds_read_b128 a[40:43], v2 offset:1472\n" \
"ds_read_b64 a[8:9], v1 offset:3008\n" \
"ds_read_b64 a[10:11], v1 offset:3016\n" \
"ds_read_b128 a[44:47], v2 offset:1984\n" \
"ds_read_b64 a[12:13], v1 offset:2496\n" \
"ds_read_b64 a[14:15], v1 offset:2504\n" \
"ds_read_b128 a[48:51], v2 offset:2496\n" \
"ds_read_b64 a[16:17], v1 offset:1984\n" \
"ds_read_b64 a[18:19], v1 offset:1992\n" \
"ds_read_b128 a[52:55], v2 offset:3008\n" \
"ds_read_b64 a[20:21], v1 offset:1472\n" \
"ds_read_b64 a[22:23], v1 offset:1480\n" \
"ds_read_b128 a[56:59], v2 offset:3520\n" \
"ds_read_b64 a[24:25], v1 offset:960\n" \
"ds_read_b64 a[26:27], v1 offset:968\n" \
"ds_read_b128 a[60:63], v2 offset:4032\n" \
"ds_read_b64 a[28:29], v1 offset:448\n" \
"ds_read_b64 a[30:31], v1 offset:456\n" \
"s_waitcnt lgkmcnt(0)\n" \
"v_mfma_f32_16x16x32_bf16 v[16:19], a[0:3], a[32:35], 0\n" \
"ds_read_b128 a[96:99], v2 offset:384\n" \
"v_lshlrev_b32 v104, 16, v94\n" \
"v_mul_f32 v104, 0x3fb8aa3b, v104\n" \
"v_mfma_f32_16x16x32_bf16 v[20:23], a[0:3], a[36:39], 0\n" \
"ds_read_b64 a[64:65], v1 offset:3968\n" \
"v_exp_f32 v104, v104\n" \
"v_lshlrev_b32 v106, 16, v88\n" \
"v_and_b32 v107, 0xffff0000, v88\n" \
"v_mfma_f32_16x16x32_bf16 v[24:27], a[0:3], a[40:43], 0\n" \
"ds_read_b64 a[66:67], v1 offset:3976\n" \
"v_lshlrev_b32 v108, 16, v89\n" \
"v_and_b32 v109, 0xffff0000, v89\n" \
"v_mfma_f32_16x16x32_bf16 v[28:31], a[0:3], a[44:47], 0\n" \
"ds_read_b128 a[100:103], v2 offset:896\n" \
"v_cvt_pk_bf16_f32 v104, v104, v104\n" \
"v_lshlrev_b32 v110, 16, v90\n" \
"v_and_b32 v111, 0xffff0000, v90\n" \
"v_mfma_f32_16x16x32_bf16 v[32:35], a[0:3], a[48:51], 0\n" \
"ds_read_b64 a[68:69], v1 offset:3456\n" \
"v_and_b32 v105, 0xffff0000, v104\n" \
"v_mov_b32 v104, v105\n" \
"v_mfma_f32_16x16x32_bf16 v[36:39], a[0:3], a[52:55], 0\n" \
"ds_read_b64 a[70:71], v1 offset:3464\n" \
"v_lshlrev_b32 v112, 16, v91\n" \
"v_and_b32 v113, 0xffff0000, v91\n" \
"v_lshlrev_b32 v114, 16, v92\n" \
"v_mfma_f32_16x16x32_bf16 v[40:43], a[0:3], a[56:59], 0\n" \
"ds_read_b128 a[104:107], v2 offset:1408\n" \
"v_and_b32 v115, 0xffff0000, v92\n" \
"v_lshlrev_b32 v116, 16, v93\n" \
"v_mfma_f32_16x16x32_bf16 v[44:47], a[0:3], a[60:63], 0\n" \
"ds_read_b64 a[72:73], v1 offset:2944\n" \
"v_and_b32 v117, 0xffff0000, v93\n" \
"v_mul_f32 v118, v106, v104\n" \
"v_mul_f32 v119, v107, v105\n" \
"v_mfma_f32_16x16x32_bf16 v[20:23], a[4:7], a[32:35], v[20:23]\n" \
"ds_read_b64 a[74:75], v1 offset:2952\n" \
"v_mul_f32 v120, v108, v104\n" \
"v_mul_f32 v121, v109, v105\n" \
"v_mfma_f32_16x16x32_bf16 v[24:27], a[4:7], a[36:39], v[24:27]\n" \
"ds_read_b128 a[108:111], v2 offset:1920\n" \
"v_mul_f32 v122, v110, v104\n" \
"v_mul_f32 v123, v111, v105\n" \
"v_mul_f32 v124, v112, v104\n" \
"v_mfma_f32_16x16x32_bf16 v[28:31], a[4:7], a[40:43], v[28:31]\n" \
"buffer_load_dword a250, v79, s[60:63], 0 offen offset:0\n" \
"ds_read_b64 a[76:77], v1 offset:2432\n" \
"v_mul_f32 v125, v113, v105\n" \
"v_mul_f32 v126, s70, v118\n" \
"v_mfma_f32_16x16x32_bf16 v[32:35], a[4:7], a[44:47], v[32:35]\n" \
"ds_read_b64 a[78:79], v1 offset:2440\n" \
"v_mul_f32 v127, s71, v119\n" \
"v_mul_f32 v128, s72, v120\n" \
"v_mul_f32 v129, s73, v121\n" \
"v_mfma_f32_16x16x32_bf16 v[36:39], a[4:7], a[48:51], v[36:39]\n" \
"ds_read_b128 a[112:115], v2 offset:2432\n" \
"v_mul_f32 v130, v114, v122\n" \
"v_mul_f32 v131, v115, v123\n" \
"v_mfma_f32_16x16x32_bf16 v[40:43], a[4:7], a[52:55], v[40:43]\n" \
"ds_read_b64 a[80:81], v1 offset:1920\n" \
"v_mul_f32 v132, v114, v124\n" \
"v_mul_f32 v133, v115, v125\n" \
"v_fma_f32 v134, -v116, v124, v130\n" \
"v_mfma_f32_16x16x32_bf16 v[44:47], a[4:7], a[56:59], v[44:47]\n" \
"ds_read_b64 a[82:83], v1 offset:1928\n" \
"v_fma_f32 v135, -v117, v125, v131\n" \
"v_fma_f32 v136, v116, v122, v132\n" \
"v_mfma_f32_16x16x32_bf16 v[24:27], a[8:11], a[32:35], v[24:27]\n" \
"ds_read_b128 a[116:119], v2 offset:2944\n" \
"v_fma_f32 v137, v117, v123, v133\n" \
"v_mul_f32 v138, s80, v126\n" \
"v_mul_f32 v139, s81, v127\n" \
"v_mfma_f32_16x16x32_bf16 v[28:31], a[8:11], a[36:39], v[28:31]\n" \
"ds_read_b64 a[84:85], v1 offset:1408\n" \
"v_mul_f32 v140, s84, v128\n" \
"v_mul_f32 v141, s85, v129\n" \
"v_mfma_f32_16x16x32_bf16 v[32:35], a[8:11], a[40:43], v[32:35]\n" \
"ds_read_b64 a[86:87], v1 offset:1416\n" \
"v_mul_f32 v142, s82, v126\n" \
"v_mul_f32 v143, s83, v127\n" \
"v_mul_f32 v144, s82, v128\n" \
"v_mfma_f32_16x16x32_bf16 v[36:39], a[8:11], a[44:47], v[36:39]\n" \
"ds_read_b128 a[120:123], v2 offset:3456\n" \
"v_mul_f32 v145, s83, v129\n" \
"v_max_f32 v146, 0, v126\n" \
"v_mfma_f32_16x16x32_bf16 v[40:43], a[8:11], a[48:51], v[40:43]\n" \
"ds_read_b64 a[88:89], v1 offset:896\n" \
"v_max_f32 v147, 0, v127\n" \
"v_exp_f32 v148, v138\n" \
"v_exp_f32 v149, v139\n" \
"v_mfma_f32_16x16x32_bf16 v[44:47], a[8:11], a[52:55], v[44:47]\n" \
"ds_read_b64 a[90:91], v1 offset:904\n" \
"v_exp_f32 v150, v126\n" \
"v_exp_f32 v151, v127\n" \
"v_mfma_f32_16x16x32_bf16 v[28:31], a[12:15], a[32:35], v[28:31]\n" \
"ds_read_b128 a[124:127], v2 offset:3968\n" \
"v_exp_f32 v152, v142\n" \
"v_exp_f32 v153, v143\n" \
"v_mul_f32 v146, 0x45000000, v146\n" \
"v_mfma_f32_16x16x32_bf16 v[32:35], a[12:15], a[36:39], v[32:35]\n" \
"ds_read_b64 a[92:93], v1 offset:384\n" \
"v_mul_f32 v147, 0x45000000, v147\n" \
"v_fract_f32 v154, v140\n" \
"v_mfma_f32_16x16x32_bf16 v[36:39], a[12:15], a[40:43], v[36:39]\n" \
"ds_read_b64 a[94:95], v1 offset:392\n" \
"v_fract_f32 v155, v141\n" \
"v_fract_f32 v156, v128\n" \
"v_fract_f32 v157, v129\n" \
"v_mfma_f32_16x16x32_bf16 v[40:43], a[12:15], a[44:47], v[40:43]\n" \
"v_fract_f32 v158, v144\n" \
"v_fract_f32 v159, v145\n" \
"v_mfma_f32_16x16x32_bf16 v[44:47], a[12:15], a[48:51], v[44:47]\n" \
"v_exp_f32 v146, v146\n" \
"v_exp_f32 v147, v147\n" \
"v_cos_f32 v160, v154\n" \
"v_mfma_f32_16x16x32_bf16 v[32:35], a[16:19], a[32:35], v[32:35]\n" \
"v_cos_f32 v161, v155\n" \
"v_cos_f32 v162, v156\n" \
"v_mfma_f32_16x16x32_bf16 v[36:39], a[16:19], a[36:39], v[36:39]\n" \
"v_cos_f32 v163, v157\n" \
"v_cos_f32 v164, v158\n" \
"v_cos_f32 v165, v159\n" \
"v_mfma_f32_16x16x32_bf16 v[40:43], a[16:19], a[40:43], v[40:43]\n" \
"v_max_f32 v166, |v134|, |v136|\n" \
"v_max_f32 v167, |v135|, |v137|\n" \
"v_mfma_f32_16x16x32_bf16 v[44:47], a[16:19], a[44:47], v[44:47]\n" \
"v_sin_f32 v168, v154\n" \
"v_sin_f32 v169, v155\n" \
"v_sin_f32 v170, v156\n" \
"v_mfma_f32_16x16x32_bf16 v[36:39], a[20:23], a[32:35], v[36:39]\n" \
"buffer_load_dword a250, v79, s[60:63], 0 offen offset:128\n" \
"v_sin_f32 v171, v157\n" \
"v_sin_f32 v172, v158\n" \
"v_mfma_f32_16x16x32_bf16 v[40:43], a[20:23], a[36:39], v[40:43]\n" \
"v_sin_f32 v173, v159\n" \
"v_mul_f32 v174, v166, v146\n" \
"v_mul_f32 v175, v167, v147\n" \
"v_mfma_f32_16x16x32_bf16 v[44:47], a[20:23], a[40:43], v[44:47]\n" \
"v_max_f32 v176, v174, v175\n" \
"v_mul_f32 v178, v148, v160\n" \
"v_mfma_f32_16x16x32_bf16 v[40:43], a[24:27], a[32:35], v[40:43]\n" \
"v_mul_f32 v179, v149, v161\n" \
"v_mul_f32 v180, v150, v162\n" \
"v_mul_f32 v181, v151, v163\n" \
"v_mfma_f32_16x16x32_bf16 v[44:47], a[24:27], a[36:39], v[44:47]\n" \
"v_mul_f32 v182, v152, v164\n" \
"v_mul_f32 v183, v153, v165\n" \
"v_mfma_f32_16x16x32_bf16 v[44:47], a[28:31], a[32:35], v[44:47]\n" \
"v_lshrrev_b32 v176, 23, v176\n" \
"v_mul_f32 v184, v148, v168\n" \
"v_mul_f32 v185, v149, v169\n" \
"s_waitcnt lgkmcnt(0)\n" \
"v_mfma_f32_16x16x32_bf16 v[16:19], a[64:67], a[96:99], v[16:19]\n" \
"ds_read_b128 a[32:35], v2 offset:320\n" \
"v_mul_f32 v186, v150, v170\n" \
"v_mul_f32 v187, v151, v171\n" \
"v_mfma_f32_16x16x32_bf16 v[20:23], a[64:67], a[100:103], v[20:23]\n" \
"ds_read_b64 a[0:1], v1 offset:3904\n" \
"v_mul_f32 v188, v152, v172\n" \
"v_mul_f32 v189, v153, v173\n" \
"v_mul_f32 v190, v178, v178\n" \
"v_mfma_f32_16x16x32_bf16 v[24:27], a[64:67], a[104:107], v[24:27]\n" \
"ds_read_b64 a[2:3], v1 offset:3912\n" \
"v_mul_f32 v191, v179, v179\n" \
"v_mul_f32 v192, v180, v180\n" \
"v_mfma_f32_16x16x32_bf16 v[28:31], a[64:67], a[108:111], v[28:31]\n" \
"ds_read_b128 a[36:39], v2 offset:832\n" \
"v_mul_f32 v193, v181, v181\n" \
"v_mul_f32 v194, v182, v182\n" \
"v_mul_f32 v195, v183, v183\n" \
"v_mfma_f32_16x16x32_bf16 v[32:35], a[64:67], a[112:115], v[32:35]\n" \
"ds_read_b64 a[4:5], v1 offset:3392\n" \
"s_nop 1\n" \
"v_max_u32_dpp v176, v176, v176 row_shr:1 row_mask:0xf bank_mask:0xf bound_ctrl:0\n" \
"v_add_f32 v196, v178, v178\n" \
"v_mfma_f32_16x16x32_bf16 v[36:39], a[64:67], a[116:119], v[36:39]\n" \
"ds_read_b64 a[6:7], v1 offset:3400\n" \
"v_add_f32 v197, v179, v179\n" \
"v_add_f32 v198, v180, v180\n" \
"v_add_f32 v199, v181, v181\n" \
"v_mfma_f32_16x16x32_bf16 v[40:43], a[64:67], a[120:123], v[40:43]\n" \
"ds_read_b128 a[40:43], v2 offset:1344\n" \
"v_add_f32 v200, v182, v182\n" \
"v_add_f32 v201, v183, v183\n" \
"v_mfma_f32_16x16x32_bf16 v[44:47], a[64:67], a[124:127], v[44:47]\n" \
"ds_read_b64 a[8:9], v1 offset:2880\n" \
"s_nop 1\n" \
"v_max_u32_dpp v176, v176, v176 row_shr:2 row_mask:0xf bank_mask:0xf bound_ctrl:0\n" \
"v_fma_f32 v202, -v184, v184, v190\n" \
"v_fma_f32 v203, -v185, v185, v191\n" \
"v_mfma_f32_16x16x32_bf16 v[20:23], a[68:71], a[96:99], v[20:23]\n" \
"ds_read_b64 a[10:11], v1 offset:2888\n" \
"v_fma_f32 v204, -v186, v186, v192\n" \
"v_fma_f32 v205, -v187, v187, v193\n" \
"v_mfma_f32_16x16x32_bf16 v[24:27], a[68:71], a[100:103], v[24:27]\n" \
"ds_read_b128 a[44:47], v2 offset:1856\n" \
"v_fma_f32 v206, -v188, v188, v194\n" \
"v_fma_f32 v207, -v189, v189, v195\n" \
"s_nop 1\n" \
"v_max_u32_dpp v176, v176, v176 row_shr:4 row_mask:0xf bank_mask:0xf bound_ctrl:0\n" \
"v_mfma_f32_16x16x32_bf16 v[28:31], a[68:71], a[104:107], v[28:31]\n" \
"ds_read_b64 a[12:13], v1 offset:2368\n" \
"v_mul_f32 v208, v196, v184\n" \
"v_mul_f32 v209, v197, v185\n" \
"v_mfma_f32_16x16x32_bf16 v[32:35], a[68:71], a[108:111], v[32:35]\n" \
"ds_read_b64 a[14:15], v1 offset:2376\n" \
"v_mul_f32 v210, v198, v186\n" \
"v_mul_f32 v211, v199, v187\n" \
"v_mul_f32 v212, v200, v188\n" \
"v_mfma_f32_16x16x32_bf16 v[36:39], a[68:71], a[112:115], v[36:39]\n" \
"ds_read_b128 a[48:51], v2 offset:2368\n" \
"v_mul_f32 v213, v201, v189\n" \
"s_nop 1\n" \
"v_max_u32_dpp v176, v176, v176 row_shr:8 row_mask:0xf bank_mask:0xf bound_ctrl:0\n" \
"v_mfma_f32_16x16x32_bf16 v[40:43], a[68:71], a[116:119], v[40:43]\n" \
"ds_read_b64 a[16:17], v1 offset:1856\n" \
"s_nop 0\n" \
"v_readlane_b32 s67, v176, 15\n" \
"v_readlane_b32 s68, v176, 31\n" \
"v_readlane_b32 s74, v176, 47\n" \
"v_mfma_f32_16x16x32_bf16 v[44:47], a[68:71], a[120:123], v[44:47]\n" \
"ds_read_b64 a[18:19], v1 offset:1864\n" \
"v_readlane_b32 s76, v176, 63\n" \
"s_max_u32 s67, s67, s68\n" \
"s_max_u32 s74, s74, s76\n" \
"s_max_u32 s67, s67, s74\n" \
"s_max_u32 s67, s67, 11\n" \
"s_min_u32 s67, s67, 254\n" \
"s_sub_u32 s74, 264, s67\n" \
"s_lshl_b32 s74, s74, 23\n" \
"s_mov_b32 s75, s74\n" \
"s_sub_u32 s76, s67, 10\n" \
"s_lshl_b32 s76, s76, 23\n" \
"v_mul_f32 v214, s74, v134\n" \
"v_mfma_f32_16x16x32_bf16 v[24:27], a[72:75], a[96:99], v[24:27]\n" \
"ds_read_b128 a[52:55], v2 offset:2880\n" \
"v_mul_f32 v215, s75, v135\n" \
"v_mul_f32 v216, s74, v136\n" \
"v_mul_f32 v217, s75, v137\n" \
"v_mfma_f32_16x16x32_bf16 v[28:31], a[72:75], a[100:103], v[28:31]\n" \
"ds_read_b64 a[20:21], v1 offset:1344\n" \
"v_mul_f32 v218, v202, v178\n" \
"v_mul_f32 v219, v203, v179\n" \
"v_mfma_f32_16x16x32_bf16 v[32:35], a[72:75], a[104:107], v[32:35]\n" \
"ds_read_b64 a[22:23], v1 offset:1352\n" \
"v_mul_f32 v220, v202, v202\n" \
"v_mul_f32 v221, v203, v203\n" \
"v_mul_f32 v222, v180, v204\n" \
"v_mfma_f32_16x16x32_bf16 v[36:39], a[72:75], a[108:111], v[36:39]\n" \
"ds_read_b128 a[56:59], v2 offset:3392\n" \
"v_mul_f32 v223, v181, v205\n" \
"v_mul_f32 v106, v204, v204\n" \
"v_mfma_f32_16x16x32_bf16 v[40:43], a[72:75], a[112:115], v[40:43]\n" \
"ds_read_b64 a[24:25], v1 offset:832\n" \
"v_mul_f32 v107, v205, v205\n" \
"v_mul_f32 v108, v206, v206\n" \
"v_mul_f32 v109, v207, v207\n" \
"v_mfma_f32_16x16x32_bf16 v[44:47], a[72:75], a[116:119], v[44:47]\n" \
"ds_read_b64 a[26:27], v1 offset:840\n" \
"v_mul_f32 v110, v214, v182\n" \
"v_mul_f32 v111, v215, v183\n" \
"v_mfma_f32_16x16x32_bf16 v[28:31], a[76:79], a[96:99], v[28:31]\n" \
"ds_read_b128 a[60:63], v2 offset:3904\n" \
"v_mul_f32 v104, v214, v206\n" \
"v_mul_f32 v105, v215, v207\n" \
"v_mul_f32 v112, v202, v184\n" \
"v_mfma_f32_16x16x32_bf16 v[32:35], a[76:79], a[100:103], v[32:35]\n" \
"ds_read_b64 a[28:29], v1 offset:320\n" \
"v_mul_f32 v113, v203, v185\n" \
"v_add_f32 v118, v202, v202\n" \
"v_mfma_f32_16x16x32_bf16 v[36:39], a[76:79], a[104:107], v[36:39]\n" \
"ds_read_b64 a[30:31], v1 offset:328\n" \
"v_add_f32 v119, v203, v203\n" \
"v_mul_f32 v120, v180, v210\n" \
"v_mul_f32 v121, v181, v211\n" \
"v_mfma_f32_16x16x32_bf16 v[40:43], a[76:79], a[108:111], v[40:43]\n" \
"v_add_f32 v114, v204, v204\n" \
"v_add_f32 v115, v205, v205\n" \
"v_mfma_f32_16x16x32_bf16 v[44:47], a[76:79], a[112:115], v[44:47]\n" \
"v_add_f32 v124, v206, v206\n" \
"v_add_f32 v125, v207, v207\n" \
"v_mul_f32 v130, v214, v188\n" \
"v_mfma_f32_16x16x32_bf16 v[32:35], a[80:83], a[96:99], v[32:35]\n" \
"v_mul_f32 v131, v215, v189\n" \
"v_mul_f32 v122, v214, v212\n" \
"v_mfma_f32_16x16x32_bf16 v[36:39], a[80:83], a[100:103], v[36:39]\n" \
"v_mul_f32 v123, v215, v213\n" \
"v_fma_f32 v132, -v208, v184, v218\n" \
"v_fma_f32 v133, -v209, v185, v219\n" \
"v_mfma_f32_16x16x32_bf16 v[40:43], a[80:83], a[104:107], v[40:43]\n" \
"v_fma_f32 v116, -v208, v208, v220\n" \
"v_fma_f32 v117, -v209, v209, v221\n" \
"v_mfma_f32_16x16x32_bf16 v[44:47], a[80:83], a[108:111], v[44:47]\n" \
"v_fma_f32 v138, -v186, v210, v222\n" \
"v_fma_f32 v139, -v187, v211, v223\n" \
"v_fma_f32 v126, -v210, v210, v106\n" \
"v_mfma_f32_16x16x32_bf16 v[36:39], a[84:87], a[96:99], v[36:39]\n" \
"buffer_load_dword a250, v79, s[60:63], 0 offen offset:256\n" \
"v_fma_f32 v127, -v211, v211, v107\n" \
"v_fma_f32 v142, -v212, v212, v108\n" \
"v_mfma_f32_16x16x32_bf16 v[40:43], a[84:87], a[100:103], v[40:43]\n" \
"v_fma_f32 v143, -v213, v213, v109\n" \
"v_fma_f32 v140, -v216, v188, v110\n" \
"v_fma_f32 v141, -v217, v189, v111\n" \
"v_mfma_f32_16x16x32_bf16 v[44:47], a[84:87], a[104:107], v[44:47]\n" \
"v_fma_f32 v128, -v216, v212, v104\n" \
"v_fma_f32 v129, -v217, v213, v105\n" \
"v_mfma_f32_16x16x32_bf16 v[40:43], a[88:91], a[96:99], v[40:43]\n" \
"v_fma_f32 v144, v208, v178, v112\n" \
"v_fma_f32 v145, v209, v179, v113\n" \
"v_mul_f32 v154, v118, v208\n" \
"v_mfma_f32_16x16x32_bf16 v[44:47], a[88:91], a[100:103], v[44:47]\n" \
"v_mul_f32 v155, v119, v209\n" \
"v_fma_f32 v156, v186, v204, v120\n" \
"v_mfma_f32_16x16x32_bf16 v[44:47], a[92:95], a[96:99], v[44:47]\n" \
"v_fma_f32 v157, v187, v205, v121\n" \
"v_mul_f32 v158, v114, v210\n" \
"v_mul_f32 v159, v115, v211\n" \
"s_waitcnt lgkmcnt(0)\n" \
"v_mfma_f32_16x16x32_bf16 v[16:19], a[0:3], a[32:35], v[16:19]\n" \
"ds_read_b128 a[96:99], v2 offset:256\n" \
"v_mul_f32 v166, v124, v212\n" \
"v_mul_f32 v167, v125, v213\n" \
"v_mfma_f32_16x16x32_bf16 v[20:23], a[0:3], a[36:39], v[20:23]\n" \
"ds_read_b64 a[64:65], v1 offset:3840\n" \
"v_fma_f32 v146, v216, v182, v130\n" \
"v_fma_f32 v147, v217, v183, v131\n" \
"v_fma_f32 v174, v216, v206, v122\n" \
"v_mfma_f32_16x16x32_bf16 v[24:27], a[0:3], a[40:43], v[24:27]\n" \
"ds_read_b64 a[66:67], v1 offset:3848\n" \
"v_fma_f32 v175, v217, v207, v123\n" \
"v_cvt_pk_f16_f32 v160, v178, v179\n" \
"v_mfma_f32_16x16x32_bf16 v[28:31], a[0:3], a[44:47], v[28:31]\n" \
"ds_read_b128 a[100:103], v2 offset:768\n" \
"v_cvt_pk_f16_f32 v161, v184, v185\n" \
"v_cvt_pk_f16_f32 v162, v202, v203\n" \
"v_cvt_pk_f16_f32 v163, v208, v209\n" \
"v_mfma_f32_16x16x32_bf16 v[32:35], a[0:3], a[48:51], v[32:35]\n" \
"ds_read_b64 a[68:69], v1 offset:3328\n" \
"v_cvt_pk_f16_f32 v164, v180, v181\n" \
"v_cvt_pk_f16_f32 v165, v186, v187\n" \
"v_mfma_f32_16x16x32_bf16 v[36:39], a[0:3], a[52:55], v[36:39]\n" \
"ds_read_b64 a[70:71], v1 offset:3336\n" \
"v_cvt_pk_f16_f32 v168, v204, v205\n" \
"v_cvt_pk_f16_f32 v169, v210, v211\n" \
"v_cvt_pk_f16_f32 v148, v214, v215\n" \
"v_mfma_f32_16x16x32_bf16 v[40:43], a[0:3], a[56:59], v[40:43]\n" \
"ds_read_b128 a[104:107], v2 offset:1280\n" \
"v_cvt_pk_f16_f32 v149, v216, v217\n" \
"v_mul_f32 v150, v116, v116\n" \
"v_mfma_f32_16x16x32_bf16 v[44:47], a[0:3], a[60:63], v[44:47]\n" \
"ds_read_b64 a[72:73], v1 offset:2816\n" \
"v_mul_f32 v151, v117, v117\n" \
"v_mul_f32 v170, v180, v126\n" \
"v_mul_f32 v171, v181, v127\n" \
"v_mfma_f32_16x16x32_bf16 v[20:23], a[4:7], a[32:35], v[20:23]\n" \
"ds_read_b64 a[74:75], v1 offset:2824\n" \
"v_mul_f32 v152, v138, v138\n" \
"v_mul_f32 v153, v139, v139\n" \
"v_mfma_f32_16x16x32_bf16 v[24:27], a[4:7], a[36:39], v[24:27]\n" \
"ds_read_b128 a[108:111], v2 offset:1792\n" \
"v_mul_f32 v172, v138, v126\n" \
"v_mul_f32 v173, v139, v127\n" \
"v_mul_f32 v190, v142, v142\n" \
"v_mfma_f32_16x16x32_bf16 v[28:31], a[4:7], a[40:43], v[28:31]\n" \
"ds_read_b64 a[76:77], v1 offset:2304\n" \
"v_mul_f32 v191, v143, v143\n" \
"v_mul_f32 v192, v140, v206\n" \
"v_mfma_f32_16x16x32_bf16 v[32:35], a[4:7], a[44:47], v[32:35]\n" \
"ds_read_b64 a[78:79], v1 offset:2312\n" \
"v_mul_f32 v193, v141, v207\n" \
"v_mul_f32 v194, v126, v142\n" \
"v_mul_f32 v195, v127, v143\n" \
"v_mfma_f32_16x16x32_bf16 v[36:39], a[4:7], a[48:51], v[36:39]\n" \
"ds_read_b128 a[112:115], v2 offset:2304\n" \
"v_add_f32 v196, v116, v116\n" \
"v_add_f32 v197, v117, v117\n" \
"v_mfma_f32_16x16x32_bf16 v[40:43], a[4:7], a[52:55], v[40:43]\n" \
"ds_read_b64 a[80:81], v1 offset:1792\n" \
"v_mul_f32 v198, v180, v158\n" \
"v_mul_f32 v199, v181, v159\n" \
"v_add_f32 v200, v138, v138\n" \
"v_mfma_f32_16x16x32_bf16 v[44:47], a[4:7], a[56:59], v[44:47]\n" \
"ds_read_b64 a[82:83], v1 offset:1800\n" \
"v_add_f32 v201, v139, v139\n" \
"v_mul_f32 v176, v138, v158\n" \
"v_mfma_f32_16x16x32_bf16 v[24:27], a[8:11], a[32:35], v[24:27]\n" \
"ds_read_b128 a[116:119], v2 offset:2816\n" \
"v_mul_f32 v177, v139, v159\n" \
"v_add_f32 v134, v142, v142\n" \
"v_add_f32 v135, v143, v143\n" \
"v_mfma_f32_16x16x32_bf16 v[28:31], a[8:11], a[36:39], v[28:31]\n" \
"ds_read_b64 a[84:85], v1 offset:1280\n" \
"v_mul_f32 v136, v140, v212\n" \
"v_mul_f32 v137, v141, v213\n" \
"v_mfma_f32_16x16x32_bf16 v[32:35], a[8:11], a[40:43], v[32:35]\n" \
"ds_read_b64 a[86:87], v1 offset:1288\n" \
"v_mul_f32 v218, v126, v166\n" \
"v_mul_f32 v219, v127, v167\n" \
"v_fma_f32 v220, -v154, v154, v150\n" \
"v_mfma_f32_16x16x32_bf16 v[36:39], a[8:11], a[44:47], v[36:39]\n" \
"ds_read_b128 a[120:123], v2 offset:3328\n" \
"v_fma_f32 v221, -v155, v155, v151\n" \
"v_fma_f32 v222, -v186, v158, v170\n" \
"v_mfma_f32_16x16x32_bf16 v[40:43], a[8:11], a[48:51], v[40:43]\n" \
"ds_read_b64 a[88:89], v1 offset:768\n" \
"v_fma_f32 v223, -v187, v159, v171\n" \
"v_fma_f32 v106, -v156, v156, v152\n" \
"v_fma_f32 v107, -v157, v157, v153\n" \
"v_mfma_f32_16x16x32_bf16 v[44:47], a[8:11], a[52:55], v[44:47]\n" \
"ds_read_b64 a[90:91], v1 offset:776\n" \
"v_fma_f32 v108, -v156, v158, v172\n" \
"v_fma_f32 v109, -v157, v159, v173\n" \
"v_mfma_f32_16x16x32_bf16 v[28:31], a[12:15], a[32:35], v[28:31]\n" \
"ds_read_b128 a[124:127], v2 offset:3840\n" \
"v_fma_f32 v188, -v166, v166, v190\n" \
"v_fma_f32 v189, -v167, v167, v191\n" \
"v_fma_f32 v110, -v146, v212, v192\n" \
"v_mfma_f32_16x16x32_bf16 v[32:35], a[12:15], a[36:39], v[32:35]\n" \
"ds_read_b64 a[92:93], v1 offset:256\n" \
"v_fma_f32 v111, -v147, v213, v193\n" \
"v_fma_f32 v104, -v158, v166, v194\n" \
"v_mfma_f32_16x16x32_bf16 v[36:39], a[12:15], a[40:43], v[36:39]\n" \
"ds_read_b64 a[94:95], v1 offset:264\n" \
"v_fma_f32 v105, -v159, v167, v195\n" \
"v_mul_f32 v112, v196, v154\n" \
"v_mul_f32 v113, v197, v155\n" \
"v_mfma_f32_16x16x32_bf16 v[40:43], a[12:15], a[44:47], v[40:43]\n" \
"v_fma_f32 v118, v186, v126, v198\n" \
"v_fma_f32 v119, v187, v127, v199\n" \
"v_mfma_f32_16x16x32_bf16 v[44:47], a[12:15], a[48:51], v[44:47]\n" \
"v_mul_f32 v120, v200, v156\n" \
"v_mul_f32 v121, v201, v157\n" \
"v_fma_f32 v114, v156, v126, v176\n" \
"v_mfma_f32_16x16x32_bf16 v[32:35], a[16:19], a[32:35], v[32:35]\n" \
"v_fma_f32 v115, v157, v127, v177\n" \
"v_mul_f32 v124, v134, v166\n" \
"v_mfma_f32_16x16x32_bf16 v[36:39], a[16:19], a[36:39], v[36:39]\n" \
"v_mul_f32 v125, v135, v167\n" \
"v_fma_f32 v182, v146, v206, v136\n" \
"v_fma_f32 v183, v147, v207, v137\n" \
"v_mfma_f32_16x16x32_bf16 v[40:43], a[16:19], a[40:43], v[40:43]\n" \
"v_fma_f32 v130, v158, v142, v218\n" \
"v_fma_f32 v131, v159, v143, v219\n" \
"v_mfma_f32_16x16x32_bf16 v[44:47], a[16:19], a[44:47], v[44:47]\n" \
"v_cvt_pk_f16_f32 v122, v132, v133\n" \
"v_cvt_pk_f16_f32 v123, v144, v145\n" \
"v_cvt_pk_f16_f32 v178, v116, v117\n" \
"v_mfma_f32_16x16x32_bf16 v[36:39], a[20:23], a[32:35], v[36:39]\n" \
"buffer_load_dword a250, v79, s[60:63], 0 offen offset:384\n" \
"v_cvt_pk_f16_f32 v179, v154, v155\n" \
"v_cvt_pk_f16_f32 v184, v138, v139\n" \
"v_mfma_f32_16x16x32_bf16 v[40:43], a[20:23], a[36:39], v[40:43]\n" \
"v_cvt_pk_f16_f32 v185, v156, v157\n" \
"v_cvt_pk_f16_f32 v202, v126, v127\n" \
"v_cvt_pk_f16_f32 v203, v158, v159\n" \
"v_mfma_f32_16x16x32_bf16 v[44:47], a[20:23], a[40:43], v[44:47]\n" \
"v_cvt_pk_f16_f32 v208, v140, v141\n" \
"v_cvt_pk_f16_f32 v209, v146, v147\n" \
"v_mfma_f32_16x16x32_bf16 v[40:43], a[24:27], a[32:35], v[40:43]\n" \
"v_cvt_pk_f16_f32 v204, v128, v129\n" \
"v_cvt_pk_f16_f32 v205, v174, v175\n" \
"v_cvt_pk_f16_f32 v210, v142, v143\n" \
"v_mfma_f32_16x16x32_bf16 v[44:47], a[24:27], a[36:39], v[44:47]\n" \
"v_cvt_pk_f16_f32 v211, v166, v167\n" \
"v_mul_f32 v214, v220, v116\n" \
"v_mfma_f32_16x16x32_bf16 v[44:47], a[28:31], a[32:35], v[44:47]\n" \
"v_mul_f32 v215, v221, v117\n" \
"v_mul_f32 v216, v188, v142\n" \
"v_mul_f32 v217, v189, v143\n" \
"s_waitcnt lgkmcnt(0)\n" \
"v_mfma_f32_16x16x32_bf16 v[16:19], a[64:67], a[96:99], v[16:19]\n" \
"ds_read_b128 a[32:35], v2 offset:192\n" \
"v_mul_f32 v180, v188, v188\n" \
"v_mul_f32 v181, v189, v189\n" \
"v_mfma_f32_16x16x32_bf16 v[20:23], a[64:67], a[100:103], v[20:23]\n" \
"ds_read_b64 a[0:1], v1 offset:3776\n" \
"v_mul_f32 v150, v220, v154\n" \
"v_mul_f32 v151, v221, v155\n" \
"v_mul_f32 v170, v188, v166\n" \
"v_mfma_f32_16x16x32_bf16 v[24:27], a[64:67], a[104:107], v[24:27]\n" \
"ds_read_b64 a[2:3], v1 offset:3784\n" \
"v_mul_f32 v171, v189, v167\n" \
"v_add_f32 v152, v188, v188\n" \
"v_mfma_f32_16x16x32_bf16 v[28:31], a[64:67], a[108:111], v[28:31]\n" \
"ds_read_b128 a[36:39], v2 offset:704\n" \
"v_add_f32 v153, v189, v189\n" \
"v_fma_f32 v172, -v112, v154, v214\n" \
"v_fma_f32 v173, -v113, v155, v215\n" \
"v_mfma_f32_16x16x32_bf16 v[32:35], a[64:67], a[112:115], v[32:35]\n" \
"ds_read_b64 a[4:5], v1 offset:3264\n" \
"v_fma_f32 v190, -v124, v166, v216\n" \
"v_fma_f32 v191, -v125, v167, v217\n" \
"v_mfma_f32_16x16x32_bf16 v[36:39], a[64:67], a[116:119], v[36:39]\n" \
"ds_read_b64 a[6:7], v1 offset:3272\n" \
"v_fma_f32 v192, -v124, v124, v180\n" \
"v_fma_f32 v193, -v125, v125, v181\n" \
"v_fma_f32 v212, v112, v116, v150\n" \
"v_mfma_f32_16x16x32_bf16 v[40:43], a[64:67], a[120:123], v[40:43]\n" \
"ds_read_b128 a[40:43], v2 offset:1216\n" \
"v_fma_f32 v213, v113, v117, v151\n" \
"v_fma_f32 v194, v124, v142, v170\n" \
"v_mfma_f32_16x16x32_bf16 v[44:47], a[64:67], a[124:127], v[44:47]\n" \
"ds_read_b64 a[8:9], v1 offset:2752\n" \
"v_fma_f32 v195, v125, v143, v171\n" \
"v_mul_f32 v196, v152, v124\n" \
"v_mul_f32 v197, v153, v125\n" \
"v_mfma_f32_16x16x32_bf16 v[20:23], a[68:71], a[96:99], v[20:23]\n" \
"ds_read_b64 a[10:11], v1 offset:2760\n" \
"v_cvt_pk_f16_f32 v198, v220, v221\n" \
"v_cvt_pk_f16_f32 v199, v112, v113\n" \
"v_mfma_f32_16x16x32_bf16 v[24:27], a[68:71], a[100:103], v[24:27]\n" \
"ds_read_b128 a[44:47], v2 offset:1728\n" \
"v_cvt_pk_f16_f32 v186, v222, v223\n" \
"v_cvt_pk_f16_f32 v187, v118, v119\n" \
"v_cvt_pk_f16_f32 v200, v106, v107\n" \
"v_mfma_f32_16x16x32_bf16 v[28:31], a[68:71], a[104:107], v[28:31]\n" \
"ds_read_b64 a[12:13], v1 offset:2240\n" \
"v_cvt_pk_f16_f32 v201, v120, v121\n" \
"v_cvt_pk_f16_f32 v176, v108, v109\n" \
"v_mfma_f32_16x16x32_bf16 v[32:35], a[68:71], a[108:111], v[32:35]\n" \
"ds_read_b64 a[14:15], v1 offset:2248\n" \
"v_cvt_pk_f16_f32 v177, v114, v115\n" \
"v_cvt_pk_f16_f32 v134, v110, v111\n" \
"v_cvt_pk_f16_f32 v135, v182, v183\n" \
"v_mfma_f32_16x16x32_bf16 v[36:39], a[68:71], a[112:115], v[36:39]\n" \
"ds_read_b128 a[48:51], v2 offset:2240\n" \
"v_cvt_pk_f16_f32 v206, v104, v105\n" \
"v_cvt_pk_f16_f32 v207, v130, v131\n" \
"v_mfma_f32_16x16x32_bf16 v[40:43], a[68:71], a[116:119], v[40:43]\n" \
"ds_read_b64 a[16:17], v1 offset:1728\n" \
"v_cvt_pk_f16_f32 v136, v188, v189\n" \
"v_cvt_pk_f16_f32 v137, v124, v125\n" \
"v_cvt_pk_f16_f32 v218, v172, v173\n" \
"v_mfma_f32_16x16x32_bf16 v[44:47], a[68:71], a[120:123], v[44:47]\n" \
"ds_read_b64 a[18:19], v1 offset:1736\n" \
"v_cvt_pk_f16_f32 v219, v212, v213\n" \
"v_cvt_pk_f16_f32 v132, v190, v191\n" \
"v_mfma_f32_16x16x32_bf16 v[24:27], a[72:75], a[96:99], v[24:27]\n" \
"ds_read_b128 a[52:55], v2 offset:2752\n" \
"v_cvt_pk_f16_f32 v133, v194, v195\n" \
"v_cvt_pk_f16_f32 v144, v192, v193\n" \
"v_cvt_pk_f16_f32 v145, v196, v197\n" \
"v_mfma_f32_16x16x32_bf16 v[28:31], a[72:75], a[100:103], v[28:31]\n" \
"ds_read_b64 a[20:21], v1 offset:1216\n" \
"v_mov_b32 v138, 0x3c003c00\n" \
"v_mov_b32 v139, 0\n" \
"v_mfma_f32_16x16x32_bf16 v[32:35], a[72:75], a[104:107], v[32:35]\n" \
"ds_read_b64 a[22:23], v1 offset:1224\n" \
"ds_write_b64 v72, v[138:139] offset:0\n" \
"v_pk_mul_f16 v156, v160, v178\n" \
"v_pk_mul_f16 v126, v162, v178\n" \
"v_mfma_f32_16x16x32_bf16 v[36:39], a[72:75], a[108:111], v[36:39]\n" \
"ds_read_b128 a[56:59], v2 offset:3264\n" \
"v_pk_mul_f16 v157, v160, v179\n" \
"v_pk_mul_f16 v127, v162, v179\n" \
"v_mfma_f32_16x16x32_bf16 v[40:43], a[72:75], a[112:115], v[40:43]\n" \
"ds_read_b64 a[24:25], v1 offset:704\n" \
"v_pk_fma_f16 v156, v161, v179, v156 neg_lo:[1,0,0] neg_hi:[1,0,0]\n" \
"v_pk_fma_f16 v126, v163, v179, v126 neg_lo:[1,0,0] neg_hi:[1,0,0]\n" \
"v_pk_fma_f16 v157, v161, v178, v157\n" \
"v_mfma_f32_16x16x32_bf16 v[44:47], a[72:75], a[116:119], v[44:47]\n" \
"ds_read_b64 a[26:27], v1 offset:712\n" \
"v_pk_fma_f16 v127, v163, v178, v127\n" \
"ds_write_b64 v72, v[156:157] offset:2640\n" \
"v_mfma_f32_16x16x32_bf16 v[28:31], a[76:79], a[96:99], v[28:31]\n" \
"ds_read_b128 a[60:63], v2 offset:3776\n" \
"ds_write_b64 v72, v[126:127] offset:3168\n" \
"ds_write_b64 v72, v[160:161] offset:528\n" \
"v_pk_mul_f16 v158, v122, v178\n" \
"v_mfma_f32_16x16x32_bf16 v[32:35], a[76:79], a[100:103], v[32:35]\n" \
"ds_read_b64 a[28:29], v1 offset:192\n" \
"v_pk_mul_f16 v140, v160, v198\n" \
"v_pk_mul_f16 v159, v122, v179\n" \
"v_mfma_f32_16x16x32_bf16 v[36:39], a[76:79], a[104:107], v[36:39]\n" \
"ds_read_b64 a[30:31], v1 offset:200\n" \
"v_pk_mul_f16 v141, v160, v199\n" \
"v_pk_fma_f16 v158, v123, v179, v158 neg_lo:[1,0,0] neg_hi:[1,0,0]\n" \
"v_pk_fma_f16 v140, v161, v199, v140 neg_lo:[1,0,0] neg_hi:[1,0,0]\n" \
"v_mfma_f32_16x16x32_bf16 v[40:43], a[76:79], a[108:111], v[40:43]\n" \
"v_pk_fma_f16 v159, v123, v178, v159\n" \
"v_pk_fma_f16 v141, v161, v198, v141\n" \
"v_mfma_f32_16x16x32_bf16 v[44:47], a[76:79], a[112:115], v[44:47]\n" \
"ds_write_b64 v72, v[158:159] offset:3696\n" \
"ds_write_b64 v72, v[140:141] offset:4752\n" \
"ds_write_b64 v72, v[162:163] offset:1056\n" \
"v_mfma_f32_16x16x32_bf16 v[32:35], a[80:83], a[96:99], v[32:35]\n" \
"v_pk_mul_f16 v146, v162, v198\n" \
"v_pk_mul_f16 v128, v122, v198\n" \
"v_mfma_f32_16x16x32_bf16 v[36:39], a[80:83], a[100:103], v[36:39]\n" \
"v_pk_mul_f16 v147, v162, v199\n" \
"v_pk_mul_f16 v129, v122, v199\n" \
"v_pk_fma_f16 v146, v163, v199, v146 neg_lo:[1,0,0] neg_hi:[1,0,0]\n" \
"v_mfma_f32_16x16x32_bf16 v[40:43], a[80:83], a[104:107], v[40:43]\n" \
"v_pk_fma_f16 v128, v123, v199, v128 neg_lo:[1,0,0] neg_hi:[1,0,0]\n" \
"v_pk_fma_f16 v147, v163, v198, v147\n" \
"v_mfma_f32_16x16x32_bf16 v[44:47], a[80:83], a[108:111], v[44:47]\n" \
"v_pk_fma_f16 v129, v123, v198, v129\n" \
"ds_write_b64 v72, v[146:147] offset:5280\n" \
"ds_write_b64 v72, v[128:129] offset:5808\n" \
"v_mfma_f32_16x16x32_bf16 v[36:39], a[84:87], a[96:99], v[36:39]\n" \
"buffer_load_dword a250, v79, s[60:63], 0 offen offset:512\n" \
"ds_write_b64 v72, v[122:123] offset:1584\n" \
"v_pk_mul_f16 v174, v160, v218\n" \
"v_mfma_f32_16x16x32_bf16 v[40:43], a[84:87], a[100:103], v[40:43]\n" \
"v_pk_mul_f16 v154, v162, v218\n" \
"v_pk_mul_f16 v175, v160, v219\n" \
"v_pk_mul_f16 v155, v162, v219\n" \
"v_mfma_f32_16x16x32_bf16 v[44:47], a[84:87], a[104:107], v[44:47]\n" \
"v_pk_fma_f16 v174, v161, v219, v174 neg_lo:[1,0,0] neg_hi:[1,0,0]\n" \
"v_pk_fma_f16 v154, v163, v219, v154 neg_lo:[1,0,0] neg_hi:[1,0,0]\n" \
"v_mfma_f32_16x16x32_bf16 v[40:43], a[88:91], a[96:99], v[40:43]\n" \
"v_pk_fma_f16 v175, v161, v218, v175\n" \
"v_pk_fma_f16 v155, v163, v218, v155\n" \
"ds_write_b64 v72, v[174:175] offset:6864\n" \
"v_mfma_f32_16x16x32_bf16 v[44:47], a[88:91], a[100:103], v[44:47]\n" \
"ds_write_b64 v72, v[154:155] offset:7392\n" \
"ds_write_b64 v72, v[178:179] offset:2112\n" \
"v_mfma_f32_16x16x32_bf16 v[44:47], a[92:95], a[96:99], v[44:47]\n" \
"v_pk_mul_f16 v214, v122, v218\n" \
"v_pk_mul_f16 v215, v122, v219\n" \
"v_pk_fma_f16 v214, v123, v219, v214 neg_lo:[1,0,0] neg_hi:[1,0,0]\n" \
"s_waitcnt lgkmcnt(0)\n" \
"v_mfma_f32_16x16x32_bf16 v[16:19], a[0:3], a[32:35], v[16:19]\n" \
"ds_read_b128 a[96:99], v2 offset:128\n" \
"v_pk_fma_f16 v215, v123, v218, v215\n" \
"ds_write_b64 v72, v[214:215] offset:7920\n" \
"v_mfma_f32_16x16x32_bf16 v[20:23], a[0:3], a[36:39], v[20:23]\n" \
"ds_read_b64 a[64:65], v1 offset:3712\n" \
"ds_write_b64 v72, v[198:199] offset:4224\n" \
"ds_write_b64 v72, v[218:219] offset:6336\n" \
"ds_read_b128 a[144:147], v73 offset:0\n" \
"v_mfma_f32_16x16x32_bf16 v[24:27], a[0:3], a[40:43], v[24:27]\n" \
"ds_read_b64 a[66:67], v1 offset:3720\n" \
"ds_read_b128 a[148:151], v73 offset:64\n" \
"ds_read_b128 a[152:155], v73 offset:128\n" \
"v_mfma_f32_16x16x32_bf16 v[28:31], a[0:3], a[44:47], v[28:31]\n" \
"ds_read_b128 a[100:103], v2 offset:640\n" \
"ds_read_b128 a[156:159], v73 offset:192\n" \
"ds_read_b128 a[160:163], v73 offset:256\n" \
"ds_read_b128 a[164:167], v73 offset:320\n" \
"v_mfma_f32_16x16x32_bf16 v[32:35], a[0:3], a[48:51], v[32:35]\n" \
"ds_read_b64 a[68:69], v1 offset:3200\n" \
"ds_read_b128 a[168:171], v73 offset:384\n" \
"ds_read_b128 a[172:175], v73 offset:448\n" \
"v_mfma_f32_16x16x32_bf16 v[36:39], a[0:3], a[52:55], v[36:39]\n" \
"ds_read_b64 a[70:71], v1 offset:3208\n" \
"ds_write_b64 v72, v[148:149] offset:0\n" \
"v_pk_mul_f16 v166, v164, v148\n" \
"v_pk_mul_f16 v216, v164, v208\n" \
"v_mfma_f32_16x16x32_bf16 v[40:43], a[0:3], a[56:59], v[40:43]\n" \
"ds_read_b128 a[104:107], v2 offset:1152\n" \
"v_pk_mul_f16 v167, v164, v149\n" \
"v_pk_mul_f16 v217, v164, v209\n" \
"v_mfma_f32_16x16x32_bf16 v[44:47], a[0:3], a[60:63], v[44:47]\n" \
"ds_read_b64 a[72:73], v1 offset:2688\n" \
"v_pk_fma_f16 v166, v165, v149, v166 neg_lo:[1,0,0] neg_hi:[1,0,0]\n" \
"v_pk_fma_f16 v216, v165, v209, v216 neg_lo:[1,0,0] neg_hi:[1,0,0]\n" \
"v_pk_fma_f16 v167, v165, v148, v167\n" \
"v_mfma_f32_16x16x32_bf16 v[20:23], a[4:7], a[32:35], v[20:23]\n" \
"ds_read_b64 a[74:75], v1 offset:2696\n" \
"v_pk_fma_f16 v217, v165, v208, v217\n" \
"ds_write_b64 v72, v[166:167] offset:528\n" \
"v_mfma_f32_16x16x32_bf16 v[24:27], a[4:7], a[36:39], v[24:27]\n" \
"ds_read_b128 a[108:111], v2 offset:1664\n" \
"ds_write_b64 v72, v[216:217] offset:4752\n" \
"ds_write_b64 v72, v[208:209] offset:4224\n" \
"v_pk_mul_f16 v180, v168, v148\n" \
"v_mfma_f32_16x16x32_bf16 v[28:31], a[4:7], a[40:43], v[28:31]\n" \
"ds_read_b64 a[76:77], v1 offset:2176\n" \
"v_pk_mul_f16 v116, v168, v208\n" \
"v_pk_mul_f16 v181, v168, v149\n" \
"v_mfma_f32_16x16x32_bf16 v[32:35], a[4:7], a[44:47], v[32:35]\n" \
"ds_read_b64 a[78:79], v1 offset:2184\n" \
"v_pk_mul_f16 v117, v168, v209\n" \
"v_pk_fma_f16 v180, v169, v149, v180 neg_lo:[1,0,0] neg_hi:[1,0,0]\n" \
"v_pk_fma_f16 v116, v169, v209, v116 neg_lo:[1,0,0] neg_hi:[1,0,0]\n" \
"v_mfma_f32_16x16x32_bf16 v[36:39], a[4:7], a[48:51], v[36:39]\n" \
"ds_read_b128 a[112:115], v2 offset:2176\n" \
"v_pk_fma_f16 v181, v169, v148, v181\n" \
"v_pk_fma_f16 v117, v169, v208, v117\n" \
"v_mfma_f32_16x16x32_bf16 v[40:43], a[4:7], a[52:55], v[40:43]\n" \
"ds_read_b64 a[80:81], v1 offset:1664\n" \
"ds_write_b64 v72, v[180:181] offset:1056\n" \
"ds_write_b64 v72, v[116:117] offset:5280\n" \
"v_pk_mul_f16 v150, v184, v148\n" \
"v_mfma_f32_16x16x32_bf16 v[44:47], a[4:7], a[56:59], v[44:47]\n" \
"ds_read_b64 a[82:83], v1 offset:1672\n" \
"v_pk_mul_f16 v170, v184, v208\n" \
"v_pk_mul_f16 v151, v184, v149\n" \
"v_mfma_f32_16x16x32_bf16 v[24:27], a[8:11], a[32:35], v[24:27]\n" \
"ds_read_b128 a[116:119], v2 offset:2688\n" \
"v_pk_mul_f16 v171, v184, v209\n" \
"v_pk_fma_f16 v150, v185, v149, v150 neg_lo:[1,0,0] neg_hi:[1,0,0]\n" \
"v_pk_fma_f16 v170, v185, v209, v170 neg_lo:[1,0,0] neg_hi:[1,0,0]\n" \
"v_mfma_f32_16x16x32_bf16 v[28:31], a[8:11], a[36:39], v[28:31]\n" \
"ds_read_b64 a[84:85], v1 offset:1152\n" \
"v_pk_fma_f16 v151, v185, v148, v151\n" \
"v_pk_fma_f16 v171, v185, v208, v171\n" \
"v_mfma_f32_16x16x32_bf16 v[32:35], a[8:11], a[40:43], v[32:35]\n" \
"ds_read_b64 a[86:87], v1 offset:1160\n" \
"ds_write_b64 v72, v[150:151] offset:1584\n" \
"ds_write_b64 v72, v[170:171] offset:5808\n" \
"v_pk_mul_f16 v142, v202, v148\n" \
"v_mfma_f32_16x16x32_bf16 v[36:39], a[8:11], a[44:47], v[36:39]\n" \
"ds_read_b128 a[120:123], v2 offset:3200\n" \
"v_pk_mul_f16 v152, v202, v208\n" \
"v_pk_mul_f16 v143, v202, v149\n" \
"v_mfma_f32_16x16x32_bf16 v[40:43], a[8:11], a[48:51], v[40:43]\n" \
"ds_read_b64 a[88:89], v1 offset:640\n" \
"v_pk_mul_f16 v153, v202, v209\n" \
"v_pk_fma_f16 v142, v203, v149, v142 neg_lo:[1,0,0] neg_hi:[1,0,0]\n" \
"v_pk_fma_f16 v152, v203, v209, v152 neg_lo:[1,0,0] neg_hi:[1,0,0]\n" \
"v_mfma_f32_16x16x32_bf16 v[44:47], a[8:11], a[52:55], v[44:47]\n" \
"ds_read_b64 a[90:91], v1 offset:648\n" \
"v_pk_fma_f16 v143, v203, v148, v143\n" \
"v_pk_fma_f16 v153, v203, v208, v153\n" \
"v_mfma_f32_16x16x32_bf16 v[28:31], a[12:15], a[32:35], v[28:31]\n" \
"ds_read_b128 a[124:127], v2 offset:3712\n" \
"ds_write_b64 v72, v[142:143] offset:2112\n" \
"ds_write_b64 v72, v[152:153] offset:6336\n" \
"v_pk_mul_f16 v220, v186, v148\n" \
"v_mfma_f32_16x16x32_bf16 v[32:35], a[12:15], a[36:39], v[32:35]\n" \
"ds_read_b64 a[92:93], v1 offset:128\n" \
"v_pk_mul_f16 v112, v186, v208\n" \
"v_pk_mul_f16 v221, v186, v149\n" \
"v_mfma_f32_16x16x32_bf16 v[36:39], a[12:15], a[40:43], v[36:39]\n" \
"ds_read_b64 a[94:95], v1 offset:136\n" \
"v_pk_mul_f16 v113, v186, v209\n" \
"v_pk_fma_f16 v220, v187, v149, v220 neg_lo:[1,0,0] neg_hi:[1,0,0]\n" \
"v_pk_fma_f16 v112, v187, v209, v112 neg_lo:[1,0,0] neg_hi:[1,0,0]\n" \
"v_mfma_f32_16x16x32_bf16 v[40:43], a[12:15], a[44:47], v[40:43]\n" \
"v_pk_fma_f16 v221, v187, v148, v221\n" \
"v_pk_fma_f16 v113, v187, v208, v113\n" \
"v_mfma_f32_16x16x32_bf16 v[44:47], a[12:15], a[48:51], v[44:47]\n" \
"ds_write_b64 v72, v[220:221] offset:2640\n" \
"ds_write_b64 v72, v[112:113] offset:6864\n" \
"v_pk_mul_f16 v222, v200, v148\n" \
"v_mfma_f32_16x16x32_bf16 v[32:35], a[16:19], a[32:35], v[32:35]\n" \
"v_pk_mul_f16 v118, v200, v208\n" \
"v_pk_mul_f16 v223, v200, v149\n" \
"v_mfma_f32_16x16x32_bf16 v[36:39], a[16:19], a[36:39], v[36:39]\n" \
"v_pk_mul_f16 v119, v200, v209\n" \
"v_pk_fma_f16 v222, v201, v149, v222 neg_lo:[1,0,0] neg_hi:[1,0,0]\n" \
"v_pk_fma_f16 v118, v201, v209, v118 neg_lo:[1,0,0] neg_hi:[1,0,0]\n" \
"v_mfma_f32_16x16x32_bf16 v[40:43], a[16:19], a[40:43], v[40:43]\n" \
"v_pk_fma_f16 v223, v201, v148, v223\n" \
"v_pk_fma_f16 v119, v201, v208, v119\n" \
"v_mfma_f32_16x16x32_bf16 v[44:47], a[16:19], a[44:47], v[44:47]\n" \
"ds_write_b64 v72, v[222:223] offset:3168\n" \
"ds_write_b64 v72, v[118:119] offset:7392\n" \
"v_pk_mul_f16 v106, v176, v148\n" \
"v_mfma_f32_16x16x32_bf16 v[36:39], a[20:23], a[32:35], v[36:39]\n" \
"buffer_load_dword a250, v79, s[60:63], 0 offen offset:640\n" \
"v_pk_mul_f16 v120, v176, v208\n" \
"v_pk_mul_f16 v107, v176, v149\n" \
"v_mfma_f32_16x16x32_bf16 v[40:43], a[20:23], a[36:39], v[40:43]\n" \
"v_pk_mul_f16 v121, v176, v209\n" \
"v_pk_fma_f16 v106, v177, v149, v106 neg_lo:[1,0,0] neg_hi:[1,0,0]\n" \
"v_pk_fma_f16 v120, v177, v209, v120 neg_lo:[1,0,0] neg_hi:[1,0,0]\n" \
"v_mfma_f32_16x16x32_bf16 v[44:47], a[20:23], a[40:43], v[44:47]\n" \
"v_pk_fma_f16 v107, v177, v148, v107\n" \
"v_pk_fma_f16 v121, v177, v208, v121\n" \
"v_mfma_f32_16x16x32_bf16 v[40:43], a[24:27], a[32:35], v[40:43]\n" \
"ds_write_b64 v72, v[106:107] offset:3696\n" \
"ds_write_b64 v72, v[120:121] offset:7920\n" \
"ds_read_b128 a[176:179], v73 offset:0\n" \
"v_mfma_f32_16x16x32_bf16 v[44:47], a[24:27], a[36:39], v[44:47]\n" \
"ds_read_b128 a[180:183], v73 offset:64\n" \
"ds_read_b128 a[184:187], v73 offset:128\n" \
"v_mfma_f32_16x16x32_bf16 v[44:47], a[28:31], a[32:35], v[44:47]\n" \
"ds_read_b128 a[188:191], v73 offset:192\n" \
"ds_read_b128 a[192:195], v73 offset:256\n" \
"ds_read_b128 a[196:199], v73 offset:320\n" \
"s_waitcnt lgkmcnt(0)\n" \
"v_mfma_f32_16x16x32_bf16 v[16:19], a[64:67], a[96:99], v[16:19]\n" \
"ds_read_b128 a[32:35], v2 offset:64\n" \
"ds_read_b128 a[200:203], v73 offset:384\n" \
"ds_read_b128 a[204:207], v73 offset:448\n" \
"v_mfma_f32_16x16x32_bf16 v[20:23], a[64:67], a[100:103], v[20:23]\n" \
"ds_read_b64 a[0:1], v1 offset:3648\n" \
"ds_write_b64 v72, v[204:205] offset:0\n" \
"v_pk_mul_f16 v108, v168, v204\n" \
"v_pk_mul_f16 v114, v168, v134\n" \
"v_mfma_f32_16x16x32_bf16 v[24:27], a[64:67], a[104:107], v[24:27]\n" \
"ds_read_b64 a[2:3], v1 offset:3656\n" \
"v_pk_mul_f16 v109, v168, v205\n" \
"v_pk_mul_f16 v115, v168, v135\n" \
"v_mfma_f32_16x16x32_bf16 v[28:31], a[64:67], a[108:111], v[28:31]\n" \
"ds_read_b128 a[36:39], v2 offset:576\n" \
"v_pk_fma_f16 v108, v169, v205, v108 neg_lo:[1,0,0] neg_hi:[1,0,0]\n" \
"v_pk_fma_f16 v114, v169, v135, v114 neg_lo:[1,0,0] neg_hi:[1,0,0]\n" \
"v_pk_fma_f16 v109, v169, v204, v109\n" \
"v_mfma_f32_16x16x32_bf16 v[32:35], a[64:67], a[112:115], v[32:35]\n" \
"ds_read_b64 a[4:5], v1 offset:3136\n" \
"v_pk_fma_f16 v115, v169, v134, v115\n" \
"ds_write_b64 v72, v[108:109] offset:528\n" \
"v_mfma_f32_16x16x32_bf16 v[36:39], a[64:67], a[116:119], v[36:39]\n" \
"ds_read_b64 a[6:7], v1 offset:3144\n" \
"ds_write_b64 v72, v[114:115] offset:2640\n" \
"ds_write_b64 v72, v[134:135] offset:2112\n" \
"v_pk_mul_f16 v110, v202, v204\n" \
"v_mfma_f32_16x16x32_bf16 v[40:43], a[64:67], a[120:123], v[40:43]\n" \
"ds_read_b128 a[40:43], v2 offset:1088\n" \
"v_pk_mul_f16 v182, v202, v134\n" \
"v_pk_mul_f16 v111, v202, v205\n" \
"v_mfma_f32_16x16x32_bf16 v[44:47], a[64:67], a[124:127], v[44:47]\n" \
"ds_read_b64 a[8:9], v1 offset:2624\n" \
"v_pk_mul_f16 v183, v202, v135\n" \
"v_pk_fma_f16 v110, v203, v205, v110 neg_lo:[1,0,0] neg_hi:[1,0,0]\n" \
"v_pk_fma_f16 v182, v203, v135, v182 neg_lo:[1,0,0] neg_hi:[1,0,0]\n" \
"v_mfma_f32_16x16x32_bf16 v[20:23], a[68:71], a[96:99], v[20:23]\n" \
"ds_read_b64 a[10:11], v1 offset:2632\n" \
"v_pk_fma_f16 v111, v203, v204, v111\n" \
"v_pk_fma_f16 v183, v203, v134, v183\n" \
"v_mfma_f32_16x16x32_bf16 v[24:27], a[68:71], a[100:103], v[24:27]\n" \
"ds_read_b128 a[44:47], v2 offset:1600\n" \
"ds_write_b64 v72, v[110:111] offset:1056\n" \
"ds_write_b64 v72, v[182:183] offset:3168\n" \
"v_pk_mul_f16 v104, v200, v204\n" \
"v_mfma_f32_16x16x32_bf16 v[28:31], a[68:71], a[104:107], v[28:31]\n" \
"ds_read_b64 a[12:13], v1 offset:2112\n" \
"v_pk_mul_f16 v130, v200, v134\n" \
"v_pk_mul_f16 v105, v200, v205\n" \
"v_mfma_f32_16x16x32_bf16 v[32:35], a[68:71], a[108:111], v[32:35]\n" \
"ds_read_b64 a[14:15], v1 offset:2120\n" \
"v_pk_mul_f16 v131, v200, v135\n" \
"v_pk_fma_f16 v104, v201, v205, v104 neg_lo:[1,0,0] neg_hi:[1,0,0]\n" \
"v_pk_fma_f16 v130, v201, v135, v130 neg_lo:[1,0,0] neg_hi:[1,0,0]\n" \
"v_mfma_f32_16x16x32_bf16 v[36:39], a[68:71], a[112:115], v[36:39]\n" \
"ds_read_b128 a[48:51], v2 offset:2112\n" \
"v_pk_fma_f16 v105, v201, v204, v105\n" \
"v_pk_fma_f16 v131, v201, v134, v131\n" \
"v_mfma_f32_16x16x32_bf16 v[40:43], a[68:71], a[116:119], v[40:43]\n" \
"ds_read_b64 a[16:17], v1 offset:1600\n" \
"ds_write_b64 v72, v[104:105] offset:1584\n" \
"ds_write_b64 v72, v[130:131] offset:3696\n" \
"v_pk_mul_f16 v188, v148, v210\n" \
"v_mfma_f32_16x16x32_bf16 v[44:47], a[68:71], a[120:123], v[44:47]\n" \
"ds_read_b64 a[18:19], v1 offset:1608\n" \
"v_pk_mul_f16 v124, v148, v206\n" \
"v_pk_mul_f16 v189, v148, v211\n" \
"v_mfma_f32_16x16x32_bf16 v[24:27], a[72:75], a[96:99], v[24:27]\n" \
"ds_read_b128 a[52:55], v2 offset:2624\n" \
"v_pk_mul_f16 v125, v148, v207\n" \
"v_pk_fma_f16 v188, v149, v211, v188 neg_lo:[1,0,0] neg_hi:[1,0,0]\n" \
"v_pk_fma_f16 v124, v149, v207, v124 neg_lo:[1,0,0] neg_hi:[1,0,0]\n" \
"v_mfma_f32_16x16x32_bf16 v[28:31], a[72:75], a[100:103], v[28:31]\n" \
"ds_read_b64 a[20:21], v1 offset:1088\n" \
"v_pk_fma_f16 v189, v149, v210, v189\n" \
"v_pk_fma_f16 v125, v149, v206, v125\n" \
"v_mfma_f32_16x16x32_bf16 v[32:35], a[72:75], a[104:107], v[32:35]\n" \
"ds_read_b64 a[22:23], v1 offset:1096\n" \
"ds_write_b64 v72, v[188:189] offset:4224\n" \
"ds_write_b64 v72, v[124:125] offset:4752\n" \
"v_pk_mul_f16 v172, v208, v210\n" \
"v_mfma_f32_16x16x32_bf16 v[36:39], a[72:75], a[108:111], v[36:39]\n" \
"ds_read_b128 a[56:59], v2 offset:3136\n" \
"v_pk_mul_f16 v212, v208, v206\n" \
"v_pk_mul_f16 v173, v208, v211\n" \
"v_mfma_f32_16x16x32_bf16 v[40:43], a[72:75], a[112:115], v[40:43]\n" \
"ds_read_b64 a[24:25], v1 offset:576\n" \
"v_pk_mul_f16 v213, v208, v207\n" \
"v_pk_fma_f16 v172, v209, v211, v172 neg_lo:[1,0,0] neg_hi:[1,0,0]\n" \
"v_pk_fma_f16 v212, v209, v207, v212 neg_lo:[1,0,0] neg_hi:[1,0,0]\n" \
"v_mfma_f32_16x16x32_bf16 v[44:47], a[72:75], a[116:119], v[44:47]\n" \
"ds_read_b64 a[26:27], v1 offset:584\n" \
"v_pk_fma_f16 v173, v209, v210, v173\n" \
"v_pk_fma_f16 v213, v209, v206, v213\n" \
"v_mfma_f32_16x16x32_bf16 v[28:31], a[76:79], a[96:99], v[28:31]\n" \
"ds_read_b128 a[60:63], v2 offset:3648\n" \
"ds_write_b64 v72, v[172:173] offset:5280\n" \
"ds_write_b64 v72, v[212:213] offset:5808\n" \
"v_pk_mul_f16 v190, v204, v210\n" \
"v_mfma_f32_16x16x32_bf16 v[32:35], a[76:79], a[100:103], v[32:35]\n" \
"ds_read_b64 a[28:29], v1 offset:64\n" \
"v_pk_mul_f16 v194, v204, v206\n" \
"v_pk_mul_f16 v191, v204, v211\n" \
"v_mfma_f32_16x16x32_bf16 v[36:39], a[76:79], a[104:107], v[36:39]\n" \
"ds_read_b64 a[30:31], v1 offset:72\n" \
"v_pk_mul_f16 v195, v204, v207\n" \
"v_pk_fma_f16 v190, v205, v211, v190 neg_lo:[1,0,0] neg_hi:[1,0,0]\n" \
"v_pk_fma_f16 v194, v205, v207, v194 neg_lo:[1,0,0] neg_hi:[1,0,0]\n" \
"v_mfma_f32_16x16x32_bf16 v[40:43], a[76:79], a[108:111], v[40:43]\n" \
"v_pk_fma_f16 v191, v205, v210, v191\n" \
"v_pk_fma_f16 v195, v205, v206, v195\n" \
"v_mfma_f32_16x16x32_bf16 v[44:47], a[76:79], a[112:115], v[44:47]\n" \
"ds_write_b64 v72, v[190:191] offset:6336\n" \
"ds_write_b64 v72, v[194:195] offset:6864\n" \
"v_pk_mul_f16 v192, v134, v210\n" \
"v_mfma_f32_16x16x32_bf16 v[32:35], a[80:83], a[96:99], v[32:35]\n" \
"v_pk_mul_f16 v196, v134, v206\n" \
"v_pk_mul_f16 v193, v134, v211\n" \
"v_mfma_f32_16x16x32_bf16 v[36:39], a[80:83], a[100:103], v[36:39]\n" \
"v_pk_mul_f16 v197, v134, v207\n" \
"v_pk_fma_f16 v192, v135, v211, v192 neg_lo:[1,0,0] neg_hi:[1,0,0]\n" \
"v_pk_fma_f16 v196, v135, v207, v196 neg_lo:[1,0,0] neg_hi:[1,0,0]\n" \
"v_mfma_f32_16x16x32_bf16 v[40:43], a[80:83], a[104:107], v[40:43]\n" \
"v_pk_fma_f16 v193, v135, v210, v193\n" \
"v_pk_fma_f16 v197, v135, v206, v197\n" \
"v_mfma_f32_16x16x32_bf16 v[44:47], a[80:83], a[108:111], v[44:47]\n" \
"ds_write_b64 v72, v[192:193] offset:7392\n" \
"ds_write_b64 v72, v[196:197] offset:7920\n" \
"ds_read_b128 a[208:211], v73 offset:0\n" \
"v_mfma_f32_16x16x32_bf16 v[36:39], a[84:87], a[96:99], v[36:39]\n" \
"buffer_load_dword a250, v79, s[60:63], 0 offen offset:768\n" \
"ds_read_b128 a[212:215], v73 offset:64\n" \
"ds_read_b128 a[216:219], v73 offset:128\n" \
"v_mfma_f32_16x16x32_bf16 v[40:43], a[84:87], a[100:103], v[40:43]\n" \
"ds_read_b128 a[220:223], v73 offset:192\n" \
"ds_read_b128 a[224:227], v73 offset:256\n" \
"ds_read_b128 a[228:231], v73 offset:320\n" \
"v_mfma_f32_16x16x32_bf16 v[44:47], a[84:87], a[104:107], v[44:47]\n" \
"ds_read_b128 a[232:235], v73 offset:384\n" \
"ds_read_b128 a[236:239], v73 offset:448\n" \
"v_mfma_f32_16x16x32_bf16 v[40:43], a[88:91], a[96:99], v[40:43]\n" \
"v_mfma_f32_16x16x32_bf16 v[44:47], a[88:91], a[100:103], v[44:47]\n" \
"v_mfma_f32_16x16x32_bf16 v[44:47], a[92:95], a[96:99], v[44:47]\n" \
"s_waitcnt lgkmcnt(0)\n" \
"v_mfma_f32_16x16x32_bf16 v[16:19], a[0:3], a[32:35], v[16:19]\n" \
"ds_read_b128 a[96:99], v2 offset:0\n" \
"v_mfma_f32_16x16x32_f16 v[156:159], a[176:179], a[144:147], 0\n" \
"v_mfma_f32_16x16x32_f16 v[156:159], a[180:183], a[148:151], v[156:159]\n" \
"v_mfma_f32_16x16x32_f16 v[156:159], a[184:187], a[152:155], v[156:159]\n" \
"v_mfma_f32_16x16x32_f16 v[156:159], a[188:191], a[156:159], v[156:159]\n" \
"v_mfma_f32_16x16x32_bf16 v[20:23], a[0:3], a[36:39], v[20:23]\n" \
"ds_read_b64 a[64:65], v1 offset:3584\n" \
"v_mfma_f32_16x16x32_f16 v[156:159], a[192:195], a[160:163], v[156:159]\n" \
"v_mfma_f32_16x16x32_f16 v[156:159], a[196:199], a[164:167], v[156:159]\n" \
"v_mfma_f32_16x16x32_bf16 v[24:27], a[0:3], a[40:43], v[24:27]\n" \
"ds_read_b64 a[66:67], v1 offset:3592\n" \
"v_mfma_f32_16x16x32_f16 v[156:159], a[200:203], a[168:171], v[156:159]\n" \
"v_mfma_f32_16x16x32_f16 v[156:159], a[204:207], a[172:175], v[156:159]\n" \
"v_pk_mul_f16 v138, v148, v136\n" \
"v_mfma_f32_16x16x32_bf16 v[28:31], a[0:3], a[44:47], v[28:31]\n" \
"ds_read_b128 a[100:103], v2 offset:512\n" \
"v_pk_mul_f16 v126, v208, v136\n" \
"v_pk_mul_f16 v139, v148, v137\n" \
"v_mfma_f32_16x16x32_bf16 v[32:35], a[0:3], a[48:51], v[32:35]\n" \
"ds_read_b64 a[68:69], v1 offset:3072\n" \
"v_pk_mul_f16 v127, v208, v137\n" \
"v_pk_fma_f16 v138, v149, v137, v138 neg_lo:[1,0,0] neg_hi:[1,0,0]\n" \
"v_pk_fma_f16 v126, v209, v137, v126 neg_lo:[1,0,0] neg_hi:[1,0,0]\n" \
"v_mfma_f32_16x16x32_bf16 v[36:39], a[0:3], a[52:55], v[36:39]\n" \
"ds_read_b64 a[70:71], v1 offset:3080\n" \
"v_pk_fma_f16 v139, v149, v136, v139\n" \
"v_pk_fma_f16 v127, v209, v136, v127\n" \
"v_mfma_f32_16x16x32_bf16 v[40:43], a[0:3], a[56:59], v[40:43]\n" \
"ds_read_b128 a[104:107], v2 offset:1024\n" \
"ds_write_b64 v72, v[138:139] offset:0\n" \
"ds_write_b64 v72, v[126:127] offset:528\n" \
"v_pk_mul_f16 v140, v204, v136\n" \
"v_mfma_f32_16x16x32_bf16 v[44:47], a[0:3], a[60:63], v[44:47]\n" \
"ds_read_b64 a[72:73], v1 offset:2560\n" \
"v_pk_mul_f16 v146, v134, v136\n" \
"v_pk_mul_f16 v141, v204, v137\n" \
"v_mfma_f32_16x16x32_bf16 v[20:23], a[4:7], a[32:35], v[20:23]\n" \
"ds_read_b64 a[74:75], v1 offset:2568\n" \
"v_pk_mul_f16 v147, v134, v137\n" \
"v_pk_fma_f16 v140, v205, v137, v140 neg_lo:[1,0,0] neg_hi:[1,0,0]\n" \
"v_pk_fma_f16 v146, v135, v137, v146 neg_lo:[1,0,0] neg_hi:[1,0,0]\n" \
"v_mfma_f32_16x16x32_bf16 v[24:27], a[4:7], a[36:39], v[24:27]\n" \
"ds_read_b128 a[108:111], v2 offset:1536\n" \
"v_pk_fma_f16 v141, v205, v136, v141\n" \
"v_pk_fma_f16 v147, v135, v136, v147\n" \
"v_mfma_f32_16x16x32_bf16 v[28:31], a[4:7], a[40:43], v[28:31]\n" \
"ds_read_b64 a[76:77], v1 offset:2048\n" \
"ds_write_b64 v72, v[140:141] offset:1056\n" \
"ds_write_b64 v72, v[146:147] offset:1584\n" \
"v_pk_mul_f16 v128, v148, v132\n" \
"v_mfma_f32_16x16x32_bf16 v[32:35], a[4:7], a[44:47], v[32:35]\n" \
"ds_read_b64 a[78:79], v1 offset:2056\n" \
"v_pk_mul_f16 v160, v208, v132\n" \
"v_pk_mul_f16 v129, v148, v133\n" \
"v_mfma_f32_16x16x32_bf16 v[36:39], a[4:7], a[48:51], v[36:39]\n" \
"ds_read_b128 a[112:115], v2 offset:2048\n" \
"v_pk_mul_f16 v161, v208, v133\n" \
"v_pk_fma_f16 v128, v149, v133, v128 neg_lo:[1,0,0] neg_hi:[1,0,0]\n" \
"v_pk_fma_f16 v160, v209, v133, v160 neg_lo:[1,0,0] neg_hi:[1,0,0]\n" \
"v_mfma_f32_16x16x32_bf16 v[40:43], a[4:7], a[52:55], v[40:43]\n" \
"ds_read_b64 a[80:81], v1 offset:1536\n" \
"v_pk_fma_f16 v129, v149, v132, v129\n" \
"v_pk_fma_f16 v161, v209, v132, v161\n" \
"v_mfma_f32_16x16x32_bf16 v[44:47], a[4:7], a[56:59], v[44:47]\n" \
"ds_read_b64 a[82:83], v1 offset:1544\n" \
"ds_write_b64 v72, v[128:129] offset:2112\n" \
"ds_write_b64 v72, v[160:161] offset:2640\n" \
"v_pk_mul_f16 v162, v204, v132\n" \
"v_mfma_f32_16x16x32_bf16 v[24:27], a[8:11], a[32:35], v[24:27]\n" \
"ds_read_b128 a[116:119], v2 offset:2560\n" \
"v_pk_mul_f16 v174, v134, v132\n" \
"v_pk_mul_f16 v163, v204, v133\n" \
"v_mfma_f32_16x16x32_bf16 v[28:31], a[8:11], a[36:39], v[28:31]\n" \
"ds_read_b64 a[84:85], v1 offset:1024\n" \
"v_pk_mul_f16 v175, v134, v133\n" \
"v_pk_fma_f16 v162, v205, v133, v162 neg_lo:[1,0,0] neg_hi:[1,0,0]\n" \
"v_pk_fma_f16 v174, v135, v133, v174 neg_lo:[1,0,0] neg_hi:[1,0,0]\n" \
"v_mfma_f32_16x16x32_bf16 v[32:35], a[8:11], a[40:43], v[32:35]\n" \
"ds_read_b64 a[86:87], v1 offset:1032\n" \
"v_pk_fma_f16 v163, v205, v132, v163\n" \
"v_pk_fma_f16 v175, v135, v132, v175\n" \
"v_mfma_f32_16x16x32_bf16 v[36:39], a[8:11], a[44:47], v[36:39]\n" \
"ds_read_b128 a[120:123], v2 offset:3072\n" \
"ds_write_b64 v72, v[162:163] offset:3168\n" \
"ds_write_b64 v72, v[174:175] offset:3696\n" \
"v_pk_mul_f16 v154, v148, v144\n" \
"v_mfma_f32_16x16x32_bf16 v[40:43], a[8:11], a[48:51], v[40:43]\n" \
"ds_read_b64 a[88:89], v1 offset:512\n" \
"v_pk_mul_f16 v155, v148, v145\n" \
"v_pk_fma_f16 v154, v149, v145, v154 neg_lo:[1,0,0] neg_hi:[1,0,0]\n" \
"v_mfma_f32_16x16x32_bf16 v[44:47], a[8:11], a[52:55], v[44:47]\n" \
"ds_read_b64 a[90:91], v1 offset:520\n" \
"v_pk_fma_f16 v155, v149, v144, v155\n" \
"ds_write_b64 v72, v[154:155] offset:4224\n" \
"ds_read_b128 a[176:179], v73 offset:0\n" \
"v_mfma_f32_16x16x32_bf16 v[28:31], a[12:15], a[32:35], v[28:31]\n" \
"ds_read_b128 a[124:127], v2 offset:3584\n" \
"ds_read_b128 a[180:183], v73 offset:64\n" \
"ds_read_b128 a[184:187], v73 offset:128\n" \
"v_mfma_f32_16x16x32_bf16 v[32:35], a[12:15], a[36:39], v[32:35]\n" \
"ds_read_b64 a[92:93], v1 offset:0\n" \
"ds_read_b128 a[188:191], v73 offset:192\n" \
"ds_read_b128 a[192:195], v73 offset:256\n" \
"ds_read_b128 a[196:199], v73 offset:320\n" \
"v_mfma_f32_16x16x32_bf16 v[36:39], a[12:15], a[40:43], v[36:39]\n" \
"ds_read_b64 a[94:95], v1 offset:8\n" \
"ds_read_b128 a[200:203], v73 offset:384\n" \
"ds_read_b128 a[204:207], v73 offset:448\n" \
"v_mfma_f32_16x16x32_bf16 v[40:43], a[12:15], a[44:47], v[40:43]\n" \
"v_mfma_f32_16x16x32_f16 v[164:167], a[208:211], a[144:147], 0\n" \
"v_mfma_f32_16x16x32_f16 v[164:167], a[212:215], a[148:151], v[164:167]\n" \
"v_mfma_f32_16x16x32_f16 v[164:167], a[216:219], a[152:155], v[164:167]\n" \
"v_mfma_f32_16x16x32_bf16 v[44:47], a[12:15], a[48:51], v[44:47]\n" \
"v_mfma_f32_16x16x32_f16 v[164:167], a[220:223], a[156:159], v[164:167]\n" \
"v_mfma_f32_16x16x32_f16 v[164:167], a[224:227], a[160:163], v[164:167]\n" \
"v_mfma_f32_16x16x32_bf16 v[32:35], a[16:19], a[32:35], v[32:35]\n" \
"v_mfma_f32_16x16x32_f16 v[164:167], a[228:231], a[164:167], v[164:167]\n" \
"v_mfma_f32_16x16x32_f16 v[164:167], a[232:235], a[168:171], v[164:167]\n" \
"v_mfma_f32_16x16x32_f16 v[164:167], a[236:239], a[172:175], v[164:167]\n" \
"v_mfma_f32_16x16x32_bf16 v[36:39], a[16:19], a[36:39], v[36:39]\n" \
"v_mfma_f32_16x16x32_bf16 v[40:43], a[16:19], a[40:43], v[40:43]\n" \
"v_mfma_f32_16x16x32_bf16 v[44:47], a[16:19], a[44:47], v[44:47]\n" \
"v_mfma_f32_16x16x32_bf16 v[36:39], a[20:23], a[32:35], v[36:39]\n" \
"buffer_load_dword a250, v79, s[60:63], 0 offen offset:896\n" \
"v_mfma_f32_16x16x32_bf16 v[40:43], a[20:23], a[36:39], v[40:43]\n" \
"v_mfma_f32_16x16x32_bf16 v[44:47], a[20:23], a[40:43], v[44:47]\n" \
"v_mfma_f32_16x16x32_bf16 v[40:43], a[24:27], a[32:35], v[40:43]\n" \
"v_mfma_f32_16x16x32_bf16 v[44:47], a[24:27], a[36:39], v[44:47]\n" \
"v_mfma_f32_16x16x32_bf16 v[44:47], a[28:31], a[32:35], v[44:47]\n" \
"s_waitcnt lgkmcnt(0)\n" \
"v_mfma_f32_16x16x32_bf16 v[16:19], a[64:67], a[96:99], v[16:19]\n" \
"v_mfma_f32_16x16x32_f16 v[216:219], a[176:179], a[144:147], 0\n" \
"v_mfma_f32_16x16x32_f16 v[216:219], a[180:183], a[148:151], v[216:219]\n" \
"v_mfma_f32_16x16x32_f16 v[216:219], a[184:187], a[152:155], v[216:219]\n" \
"v_mfma_f32_16x16x32_f16 v[216:219], a[188:191], a[156:159], v[216:219]\n" \
"v_mfma_f32_16x16x32_bf16 v[20:23], a[64:67], a[100:103], v[20:23]\n" \
"v_mfma_f32_16x16x32_f16 v[216:219], a[192:195], a[160:163], v[216:219]\n" \
"v_mfma_f32_16x16x32_f16 v[216:219], a[196:199], a[164:167], v[216:219]\n" \
"v_mfma_f32_16x16x32_bf16 v[24:27], a[64:67], a[104:107], v[24:27]\n" \
"v_mfma_f32_16x16x32_f16 v[216:219], a[200:203], a[168:171], v[216:219]\n" \
"v_mfma_f32_16x16x32_f16 v[216:219], a[204:207], a[172:175], v[216:219]\n" \
"v_add_u32 v178, s88, v48\n" \
"v_mfma_f32_16x16x32_bf16 v[28:31], a[64:67], a[108:111], v[28:31]\n" \
"v_add_u32 v179, s88, v49\n" \
"v_add_u32 v122, s88, v50\n" \
"v_mfma_f32_16x16x32_bf16 v[32:35], a[64:67], a[112:115], v[32:35]\n" \
"v_add_u32 v123, s88, v51\n" \
"v_fma_f32 v214, v156, s76, v95\n" \
"v_mul_f32 v215, s76, v157\n" \
"v_mfma_f32_16x16x32_bf16 v[36:39], a[64:67], a[116:119], v[36:39]\n" \
"v_cvt_pk_bf16_f32 v214, v214, v215\n" \
"v_mul_f32 v198, s76, v158\n" \
"v_mfma_f32_16x16x32_bf16 v[40:43], a[64:67], a[120:123], v[40:43]\n" \
"v_mul_f32 v199, s76, v159\n" \
"v_cvt_pk_bf16_f32 v198, v198, v199\n" \
"v_mul_f32 v180, s76, v164\n" \
"v_mfma_f32_16x16x32_bf16 v[44:47], a[64:67], a[124:127], v[44:47]\n" \
"v_mul_f32 v181, s76, v165\n" \
"v_cvt_pk_bf16_f32 v180, v180, v181\n" \
"v_mfma_f32_16x16x32_bf16 v[20:23], a[68:71], a[96:99], v[20:23]\n" \
"v_mul_f32 v116, s76, v166\n" \
"v_mul_f32 v117, s76, v167\n" \
"v_cvt_pk_bf16_f32 v116, v116, v117\n" \
"v_mfma_f32_16x16x32_bf16 v[24:27], a[68:71], a[100:103], v[24:27]\n" \
"ds_write_b32 v178, v214\n" \
"ds_write_b32 v178, v198 offset:4\n" \
"v_mfma_f32_16x16x32_bf16 v[28:31], a[68:71], a[104:107], v[28:31]\n" \
"ds_write_b32 v179, v180\n" \
"ds_write_b32 v122, v116\n" \
"s_nop 1\n" \
"v_mul_f32 v184, s76, v216\n" \
"v_mfma_f32_16x16x32_bf16 v[32:35], a[68:71], a[108:111], v[32:35]\n" \
"v_mul_f32 v185, s76, v217\n" \
"v_cvt_pk_bf16_f32 v184, v184, v185\n" \
"v_mfma_f32_16x16x32_bf16 v[36:39], a[68:71], a[112:115], v[36:39]\n" \
"v_mul_f32 v150, s76, v218\n" \
"v_mul_f32 v151, s76, v219\n" \
"v_cvt_pk_bf16_f32 v150, v150, v151\n" \
"v_mfma_f32_16x16x32_bf16 v[40:43], a[68:71], a[116:119], v[40:43]\n" \
"ds_write_b16 v123, v184\n" \
"ds_write_b16_d16_hi v123, v184 offset:32\n" \
"v_mfma_f32_16x16x32_bf16 v[44:47], a[68:71], a[120:123], v[44:47]\n" \
"ds_write_b16 v123, v150 offset:64\n" \
"ds_write_b16_d16_hi v123, v150 offset:96\n" \
"v_mfma_f32_16x16x32_bf16 v[24:27], a[72:75], a[96:99], v[24:27]\n" \
"v_mfma_f32_16x16x32_bf16 v[28:31], a[72:75], a[100:103], v[28:31]\n" \
"v_mfma_f32_16x16x32_bf16 v[32:35], a[72:75], a[104:107], v[32:35]\n" \
"v_mfma_f32_16x16x32_bf16 v[36:39], a[72:75], a[108:111], v[36:39]\n" \
"v_mfma_f32_16x16x32_bf16 v[40:43], a[72:75], a[112:115], v[40:43]\n" \
"v_mfma_f32_16x16x32_bf16 v[44:47], a[72:75], a[116:119], v[44:47]\n" \
"v_mfma_f32_16x16x32_bf16 v[28:31], a[76:79], a[96:99], v[28:31]\n" \
"v_mfma_f32_16x16x32_bf16 v[32:35], a[76:79], a[100:103], v[32:35]\n" \
"v_mfma_f32_16x16x32_bf16 v[36:39], a[76:79], a[104:107], v[36:39]\n" \
"v_mfma_f32_16x16x32_bf16 v[40:43], a[76:79], a[108:111], v[40:43]\n" \
"v_mfma_f32_16x16x32_bf16 v[44:47], a[76:79], a[112:115], v[44:47]\n" \
"v_mfma_f32_16x16x32_bf16 v[32:35], a[80:83], a[96:99], v[32:35]\n" \
"v_mfma_f32_16x16x32_bf16 v[36:39], a[80:83], a[100:103], v[36:39]\n" \
"v_mfma_f32_16x16x32_bf16 v[40:43], a[80:83], a[104:107], v[40:43]\n" \
"v_mfma_f32_16x16x32_bf16 v[44:47], a[80:83], a[108:111], v[44:47]\n" \
"v_mfma_f32_16x16x32_bf16 v[36:39], a[84:87], a[96:99], v[36:39]\n" \
"buffer_load_dword a250, v79, s[60:63], 0 offen offset:1024\n" \
"v_mfma_f32_16x16x32_bf16 v[40:43], a[84:87], a[100:103], v[40:43]\n" \
"v_mfma_f32_16x16x32_bf16 v[44:47], a[84:87], a[104:107], v[44:47]\n" \
"v_mfma_f32_16x16x32_bf16 v[40:43], a[88:91], a[96:99], v[40:43]\n" \
"v_mfma_f32_16x16x32_bf16 v[44:47], a[88:91], a[100:103], v[44:47]\n" \
"v_mfma_f32_16x16x32_bf16 v[44:47], a[92:95], a[96:99], v[44:47]\n" \
"s_nop 7\n" \
"s_getpc_b64 s[64:65]\n" \
"Lr4_%=:\n" \
"s_add_u32 s64, s64, Lb4_%=-Lr4_%=\n" \
"s_addc_u32 s65, s65, 0\n" \
"s_branch Lst_%=\n" \
"Lb4_%=:\n" \
"s_waitcnt vmcnt(13)\n" \
"s_cmp_lg_u32 s40, 1\n" \
"s_cbranch_scc1 Le1_%=\n" \
"v_mov_b32 v3, 156048\n" \
"s_mov_b32 s68, 0x400000\n" \
"Lw1_%=:\n" \
"ds_read_b32 v4, v3\n" \
"s_waitcnt lgkmcnt(0)\n" \
"v_readfirstlane_b32 s67, v4\n" \
"s_sub_u32 s68, s68, 1\n" \
"s_cbranch_scc1 Lx1_%=\n" \
"s_cmp_eq_u32 s67, 0\n" \
"s_cbranch_scc1 Lw1_%=\n" \
"Lx1_%=:\n" \
"s_sub_u32 s88, 116608, s41\n" \
"s_sub_u32 s67, 116608, s41\n" \
"s_getpc_b64 s[64:65]\n" \
"Lr5_%=:\n" \
"s_add_u32 s64, s64, Lb5_%=-Lr5_%=\n" \
"s_addc_u32 s65, s65, 0\n" \
"s_branch Lex_%=\n" \
"Lb5_%=:\n" \
"v_add_u32 v3, 143248, v78\n" \
"ds_write_b128 v3, a[128:131] offset:512\n" \
"ds_write_b128 v3, a[132:135] offset:1536\n" \
"ds_write_b128 v3, a[136:139] offset:2560\n" \
"ds_write_b128 v3, a[140:143] offset:3584\n" \
"s_waitcnt lgkmcnt(0)\n" \
"v_mov_b32 v3, 156052\n" \
"v_mov_b32 v4, 1\n" \
"ds_write_b32 v3, v4\n" \
"s_mov_b32 s88, 0\n" \
"Le1_%=:\n" \
"s_mov_b32 s67, 0\n" \
"s_getpc_b64 s[64:65]\n" \
"Lr6_%=:\n" \
"s_add_u32 s64, s64, Lb6_%=-Lr6_%=\n" \
"s_addc_u32 s65, s65, 0\n" \
"s_branch Lex_%=\n" \
"Lb6_%=:\n" \
"s_add_u32 s67, s41, 26640\n" \
"v_add_u32 v3, s67, v78\n" \
"ds_write_b128 v3, v[240:243] offset:512\n" \
"ds_write_b128 v3, v[244:247] offset:1536\n" \
"ds_write_b128 v3, v[248:251] offset:2560\n" \
"ds_write_b128 v3, v[252:255] offset:3584\n" \
"v_mov_b32 v1, v69\n" \
"v_mov_b32 v2, v70\n" \
"s_mov_b32 s77, 4\n" \
"s_add_u32 s87, s87, 4\n" \
"s_getpc_b64 s[64:65]\n" \
"Lr7_%=:\n" \
"s_add_u32 s64, s64, Lb7_%=-Lr7_%=\n" \
"s_addc_u32 s65, s65, 0\n" \
"s_branch Lcl_%=\n" \
"Lb7_%=:\n" \
"s_getpc_b64 s[64:65]\n" \
"Lr8_%=:\n" \
"s_add_u32 s64, s64, Lb8_%=-Lr8_%=\n" \
"s_addc_u32 s65, s65, 0\n" \
"s_branch Lst_%=\n" \
"Lb8_%=:\n" \
"s_cmp_lt_u32 s40, 2\n" \
"s_cbranch_scc1 Lend_%=\n" \
"v_mov_b32 v3, 156052\n" \
"s_mov_b32 s68, 0x400000\n" \
"Lw2_%=:\n" \
"ds_read_b32 v4, v3\n" \
"s_waitcnt lgkmcnt(0)\n" \
"v_readfirstlane_b32 s67, v4\n" \
"s_sub_u32 s68, s68, 1\n" \
"s_cbranch_scc1 Lx2_%=\n" \
"s_cmp_eq_u32 s67, 0\n" \
"s_cbranch_scc1 Lw2_%=\n" \
"Lx2_%=:\n" \
"s_sub_u32 s67, s40, 2\n" \
"s_lshl_b32 s67, s67, 8\n" \
"s_add_u32 s67, s67, s41\n" \
"s_sub_u32 s68, 116608, s67\n" \
"v_add_u32 v1, s68, v69\n" \
"s_sub_u32 s68, 116608, s67\n" \
"v_add_u32 v2, s68, v70\n" \
"s_mov_b32 s77, 2\n" \
"s_add_u32 s87, s55, 8\n" \
"s_getpc_b64 s[64:65]\n" \
"Lr9_%=:\n" \
"s_add_u32 s64, s64, Lb9_%=-Lr9_%=\n" \
"s_addc_u32 s65, s65, 0\n" \
"s_branch Lcl_%=\n" \
"Lb9_%=:\n" \
"v_add_u32 v5, 147856, v78\n" \
"s_cmp_eq_u32 s40, 2\n" \
"s_cbranch_scc1 Lq2_%=\n" \
"ds_write_b128 v5, v[16:19] offset:0\n" \
"ds_write_b128 v5, v[20:23] offset:1024\n" \
"ds_write_b128 v5, v[24:27] offset:2048\n" \
"ds_write_b128 v5, v[28:31] offset:3072\n" \
"ds_write_b128 v5, v[32:35] offset:4096\n" \
"ds_write_b128 v5, v[36:39] offset:5120\n" \
"ds_write_b128 v5, v[40:43] offset:6144\n" \
"ds_write_b128 v5, v[44:47] offset:7168\n" \
"s_waitcnt lgkmcnt(0)\n" \
"v_mov_b32 v3, 156056\n" \
"v_mov_b32 v4, 1\n" \
"ds_write_b32 v3, v4\n" \
"s_branch Lend_%=\n" \
"Lq2_%=:\n" \
"v_mov_b32 v3, 156056\n" \
"s_mov_b32 s68, 0x400000\n" \
"Lw3_%=:\n" \
"ds_read_b32 v4, v3\n" \
"s_waitcnt lgkmcnt(0)\n" \
"v_readfirstlane_b32 s67, v4\n" \
"s_sub_u32 s68, s68, 1\n" \
"s_cbranch_scc1 Lx3_%=\n" \
"s_cmp_eq_u32 s67, 0\n" \
"s_cbranch_scc1 Lw3_%=\n" \
"Lx3_%=:\n" \
"ds_read_b128 v[136:139], v5 offset:0\n" \
"ds_read_b128 v[140:143], v5 offset:1024\n" \
"ds_read_b128 v[144:147], v5 offset:2048\n" \
"ds_read_b128 v[148:151], v5 offset:3072\n" \
"ds_read_b128 v[152:155], v5 offset:4096\n" \
"ds_read_b128 v[156:159], v5 offset:5120\n" \
"ds_read_b128 v[160:163], v5 offset:6144\n" \
"ds_read_b128 v[164:167], v5 offset:7168\n" \
"s_waitcnt lgkmcnt(0)\n" \
"v_add_f32 v16, v16, v136\n" \
"v_add_f32 v17, v17, v137\n" \
"v_add_f32 v18, v18, v138\n" \
"v_add_f32 v19, v19, v139\n" \
"v_add_f32 v20, v20, v140\n" \
"v_add_f32 v21, v21, v141\n" \
"v_add_f32 v22, v22, v142\n" \
"v_add_f32 v23, v23, v143\n" \
"v_add_f32 v24, v24, v144\n" \
"v_add_f32 v25, v25, v145\n" \
"v_add_f32 v26, v26, v146\n" \
"v_add_f32 v27, v27, v147\n" \
"v_add_f32 v28, v28, v148\n" \
"v_add_f32 v29, v29, v149\n" \
"v_add_f32 v30, v30, v150\n" \
"v_add_f32 v31, v31, v151\n" \
"v_add_f32 v32, v32, v152\n" \
"v_add_f32 v33, v33, v153\n" \
"v_add_f32 v34, v34, v154\n" \
"v_add_f32 v35, v35, v155\n" \
"v_add_f32 v36, v36, v156\n" \
"v_add_f32 v37, v37, v157\n" \
"v_add_f32 v38, v38, v158\n" \
"v_add_f32 v39, v39, v159\n" \
"v_add_f32 v40, v40, v160\n" \
"v_add_f32 v41, v41, v161\n" \
"v_add_f32 v42, v42, v162\n" \
"v_add_f32 v43, v43, v163\n" \
"v_add_f32 v44, v44, v164\n" \
"v_add_f32 v45, v45, v165\n" \
"v_add_f32 v46, v46, v166\n" \
"v_add_f32 v47, v47, v167\n" \
"s_getpc_b64 s[64:65]\n" \
"Lr10_%=:\n" \
"s_add_u32 s64, s64, Lb10_%=-Lr10_%=\n" \
"s_addc_u32 s65, s65, 0\n" \
"s_branch Lst_%=\n" \
"Lb10_%=:\n" \
"s_branch Lend_%=\n" \
"Lkg_%=:\n" \
"v_lshlrev_b32 v104, 16, v246\n" \
"v_mul_f32 v104, 0x3fb8aa3b, v104\n" \
"v_exp_f32 v104, v104\n" \
"v_lshlrev_b32 v106, 16, v240\n" \
"v_and_b32 v107, 0xffff0000, v240\n" \
"v_lshlrev_b32 v108, 16, v241\n" \
"v_and_b32 v109, 0xffff0000, v241\n" \
"v_cvt_pk_bf16_f32 v104, v104, v104\n" \
"v_lshlrev_b32 v110, 16, v242\n" \
"v_and_b32 v111, 0xffff0000, v242\n" \
"v_and_b32 v105, 0xffff0000, v104\n" \
"v_mov_b32 v104, v105\n" \
"v_lshlrev_b32 v112, 16, v243\n" \
"v_and_b32 v113, 0xffff0000, v243\n" \
"v_lshlrev_b32 v114, 16, v244\n" \
"v_and_b32 v115, 0xffff0000, v244\n" \
"v_lshlrev_b32 v116, 16, v245\n" \
"v_and_b32 v117, 0xffff0000, v245\n" \
"v_pk_mul_f32 v[118:119], v[106:107], v[104:105]\n" \
"v_pk_mul_f32 v[120:121], v[108:109], v[104:105]\n" \
"v_pk_mul_f32 v[122:123], v[110:111], v[104:105]\n" \
"v_pk_mul_f32 v[124:125], v[112:113], v[104:105]\n" \
"v_pk_mul_f32 v[126:127], v[118:119], s[70:71]\n" \
"v_pk_mul_f32 v[128:129], v[120:121], s[72:73]\n" \
"v_pk_mul_f32 v[130:131], v[114:115], v[122:123]\n" \
"v_pk_mul_f32 v[132:133], v[114:115], v[124:125]\n" \
"v_pk_fma_f32 v[134:135], v[116:117], v[124:125], v[130:131] neg_lo:[1,0,0] neg_hi:[1,0,0]\n" \
"v_pk_fma_f32 v[136:137], v[116:117], v[122:123], v[132:133]\n" \
"v_pk_mul_f32 v[138:139], v[126:127], s[80:81]\n" \
"v_pk_mul_f32 v[140:141], v[128:129], s[84:85]\n" \
"v_pk_mul_f32 v[142:143], v[126:127], s[82:83]\n" \
"v_pk_mul_f32 v[144:145], v[128:129], s[82:83]\n" \
"v_max_f32 v146, 0, v126\n" \
"v_max_f32 v147, 0, v127\n" \
"v_exp_f32 v148, v138\n" \
"v_exp_f32 v149, v139\n" \
"v_exp_f32 v150, v126\n" \
"v_exp_f32 v151, v127\n" \
"v_exp_f32 v152, v142\n" \
"v_exp_f32 v153, v143\n" \
"v_mul_f32 v146, 0x45000000, v146\n" \
"v_mul_f32 v147, 0x45000000, v147\n" \
"v_fract_f32 v154, v140\n" \
"v_fract_f32 v155, v141\n" \
"v_fract_f32 v156, v128\n" \
"v_fract_f32 v157, v129\n" \
"v_fract_f32 v158, v144\n" \
"v_fract_f32 v159, v145\n" \
"v_exp_f32 v146, v146\n" \
"v_exp_f32 v147, v147\n" \
"v_cos_f32 v160, v154\n" \
"v_cos_f32 v161, v155\n" \
"v_cos_f32 v162, v156\n" \
"v_cos_f32 v163, v157\n" \
"v_cos_f32 v164, v158\n" \
"v_cos_f32 v165, v159\n" \
"v_max_f32 v166, |v134|, |v136|\n" \
"v_max_f32 v167, |v135|, |v137|\n" \
"v_sin_f32 v168, v154\n" \
"v_sin_f32 v169, v155\n" \
"v_sin_f32 v170, v156\n" \
"v_sin_f32 v171, v157\n" \
"v_sin_f32 v172, v158\n" \
"v_sin_f32 v173, v159\n" \
"v_pk_mul_f32 v[174:175], v[166:167], v[146:147]\n" \
"v_max_f32 v176, v174, v175\n" \
"v_pk_mul_f32 v[178:179], v[148:149], v[160:161]\n" \
"v_pk_mul_f32 v[180:181], v[150:151], v[162:163]\n" \
"v_pk_mul_f32 v[182:183], v[152:153], v[164:165]\n" \
"v_lshrrev_b32 v176, 23, v176\n" \
"v_pk_mul_f32 v[184:185], v[148:149], v[168:169]\n" \
"v_pk_mul_f32 v[186:187], v[150:151], v[170:171]\n" \
"v_pk_mul_f32 v[188:189], v[152:153], v[172:173]\n" \
"v_pk_mul_f32 v[190:191], v[178:179], v[178:179]\n" \
"v_pk_mul_f32 v[192:193], v[180:181], v[180:181]\n" \
"v_pk_mul_f32 v[194:195], v[182:183], v[182:183]\n" \
"s_nop 1\n" \
"v_max_u32_dpp v176, v176, v176 row_shr:1 row_mask:0xf bank_mask:0xf bound_ctrl:0\n" \
"v_pk_add_f32 v[196:197], v[178:179], v[178:179]\n" \
"v_pk_add_f32 v[198:199], v[180:181], v[180:181]\n" \
"v_pk_add_f32 v[200:201], v[182:183], v[182:183]\n" \
"s_nop 1\n" \
"v_max_u32_dpp v176, v176, v176 row_shr:2 row_mask:0xf bank_mask:0xf bound_ctrl:0\n" \
"v_pk_fma_f32 v[202:203], v[184:185], v[184:185], v[190:191] neg_lo:[1,0,0] neg_hi:[1,0,0]\n" \
"v_pk_fma_f32 v[204:205], v[186:187], v[186:187], v[192:193] neg_lo:[1,0,0] neg_hi:[1,0,0]\n" \
"v_pk_fma_f32 v[206:207], v[188:189], v[188:189], v[194:195] neg_lo:[1,0,0] neg_hi:[1,0,0]\n" \
"s_nop 1\n" \
"v_max_u32_dpp v176, v176, v176 row_shr:4 row_mask:0xf bank_mask:0xf bound_ctrl:0\n" \
"v_pk_mul_f32 v[208:209], v[196:197], v[184:185]\n" \
"v_pk_mul_f32 v[210:211], v[198:199], v[186:187]\n" \
"v_pk_mul_f32 v[212:213], v[200:201], v[188:189]\n" \
"s_nop 1\n" \
"v_max_u32_dpp v176, v176, v176 row_shr:8 row_mask:0xf bank_mask:0xf bound_ctrl:0\n" \
"s_nop 0\n" \
"v_readlane_b32 s67, v176, 15\n" \
"v_readlane_b32 s68, v176, 31\n" \
"v_readlane_b32 s74, v176, 47\n" \
"v_readlane_b32 s76, v176, 63\n" \
"s_max_u32 s67, s67, s68\n" \
"s_max_u32 s74, s74, s76\n" \
"s_max_u32 s67, s67, s74\n" \
"s_max_u32 s67, s67, 11\n" \
"s_min_u32 s67, s67, 254\n" \
"s_sub_u32 s74, 264, s67\n" \
"s_lshl_b32 s74, s74, 23\n" \
"s_mov_b32 s75, s74\n" \
"s_sub_u32 s76, s67, 10\n" \
"s_lshl_b32 s76, s76, 23\n" \
"v_pk_mul_f32 v[214:215], v[134:135], s[74:75]\n" \
"v_pk_mul_f32 v[216:217], v[136:137], s[74:75]\n" \
"v_pk_mul_f32 v[218:219], v[202:203], v[178:179]\n" \
"v_pk_mul_f32 v[220:221], v[202:203], v[202:203]\n" \
"v_pk_mul_f32 v[222:223], v[180:181], v[204:205]\n" \
"v_pk_mul_f32 v[106:107], v[204:205], v[204:205]\n" \
"v_pk_mul_f32 v[108:109], v[206:207], v[206:207]\n" \
"v_pk_mul_f32 v[110:111], v[214:215], v[182:183]\n" \
"v_pk_mul_f32 v[104:105], v[214:215], v[206:207]\n" \
"v_pk_mul_f32 v[112:113], v[202:203], v[184:185]\n" \
"v_pk_add_f32 v[118:119], v[202:203], v[202:203]\n" \
"v_pk_mul_f32 v[120:121], v[180:181], v[210:211]\n" \
"v_pk_add_f32 v[114:115], v[204:205], v[204:205]\n" \
"v_pk_add_f32 v[124:125], v[206:207], v[206:207]\n" \
"v_pk_mul_f32 v[130:131], v[214:215], v[188:189]\n" \
"v_pk_mul_f32 v[122:123], v[214:215], v[212:213]\n" \
"v_pk_fma_f32 v[132:133], v[208:209], v[184:185], v[218:219] neg_lo:[1,0,0] neg_hi:[1,0,0]\n" \
"v_pk_fma_f32 v[116:117], v[208:209], v[208:209], v[220:221] neg_lo:[1,0,0] neg_hi:[1,0,0]\n" \
"v_pk_fma_f32 v[138:139], v[186:187], v[210:211], v[222:223] neg_lo:[1,0,0] neg_hi:[1,0,0]\n" \
"v_pk_fma_f32 v[126:127], v[210:211], v[210:211], v[106:107] neg_lo:[1,0,0] neg_hi:[1,0,0]\n" \
"v_pk_fma_f32 v[142:143], v[212:213], v[212:213], v[108:109] neg_lo:[1,0,0] neg_hi:[1,0,0]\n" \
"v_pk_fma_f32 v[140:141], v[216:217], v[188:189], v[110:111] neg_lo:[1,0,0] neg_hi:[1,0,0]\n" \
"v_pk_fma_f32 v[128:129], v[216:217], v[212:213], v[104:105] neg_lo:[1,0,0] neg_hi:[1,0,0]\n" \
"v_pk_fma_f32 v[144:145], v[208:209], v[178:179], v[112:113]\n" \
"v_pk_mul_f32 v[154:155], v[118:119], v[208:209]\n" \
"v_pk_fma_f32 v[156:157], v[186:187], v[204:205], v[120:121]\n" \
"v_pk_mul_f32 v[158:159], v[114:115], v[210:211]\n" \
"v_pk_mul_f32 v[166:167], v[124:125], v[212:213]\n" \
"v_pk_fma_f32 v[146:147], v[216:217], v[182:183], v[130:131]\n" \
"v_pk_fma_f32 v[174:175], v[216:217], v[206:207], v[122:123]\n" \
"v_cvt_pk_f16_f32 v160, v178, v179\n" \
"v_cvt_pk_f16_f32 v161, v184, v185\n" \
"v_cvt_pk_f16_f32 v162, v202, v203\n" \
"v_cvt_pk_f16_f32 v163, v208, v209\n" \
"v_cvt_pk_f16_f32 v164, v180, v181\n" \
"v_cvt_pk_f16_f32 v165, v186, v187\n" \
"v_cvt_pk_f16_f32 v168, v204, v205\n" \
"v_cvt_pk_f16_f32 v169, v210, v211\n" \
"v_cvt_pk_f16_f32 v148, v214, v215\n" \
"v_cvt_pk_f16_f32 v149, v216, v217\n" \
"s_cmp_eq_u32 s66, 0\n" \
"s_cbranch_scc1 Lhx_%=\n" \
"s_mov_b32 s66, 0\n" \
"s_waitcnt vmcnt(2)\n" \
"v_mov_b32 v3, 156060\n" \
"v_mov_b32 v4, 1\n" \
"s_mov_b64 exec, 1\n" \
"ds_add_rtn_u32 v4, v3, v4\n" \
"s_waitcnt lgkmcnt(0)\n" \
"v_readfirstlane_b32 s67, v4\n" \
"s_cmp_lg_u32 s67, 3\n" \
"s_cbranch_scc1 Lhy_%=\n" \
"s_lshl_b32 s67, s56, 2\n" \
"s_add_u32 s67, s67, 128\n" \
"v_mov_b32 v3, s67\n" \
"v_mov_b32 v4, s54\n" \
"global_store_dword v3, v4, s[52:53] sc1\n" \
"s_lshl_b32 s67, s57, 2\n" \
"s_add_u32 s67, s67, 393856\n" \
"v_mov_b32 v5, s67\n" \
"global_store_dword v5, v4, s[52:53] sc1\n" \
"Lhy_%=:\n" \
"s_mov_b64 exec, -1\n" \
"Lhx_%=:\n" \
"v_pk_mul_f32 v[150:151], v[116:117], v[116:117]\n" \
"v_pk_mul_f32 v[170:171], v[180:181], v[126:127]\n" \
"v_pk_mul_f32 v[152:153], v[138:139], v[138:139]\n" \
"v_pk_mul_f32 v[172:173], v[138:139], v[126:127]\n" \
"v_pk_mul_f32 v[190:191], v[142:143], v[142:143]\n" \
"v_pk_mul_f32 v[192:193], v[140:141], v[206:207]\n" \
"v_pk_mul_f32 v[194:195], v[126:127], v[142:143]\n" \
"v_pk_add_f32 v[196:197], v[116:117], v[116:117]\n" \
"v_pk_mul_f32 v[198:199], v[180:181], v[158:159]\n" \
"v_pk_add_f32 v[200:201], v[138:139], v[138:139]\n" \
"v_pk_mul_f32 v[176:177], v[138:139], v[158:159]\n" \
"v_pk_add_f32 v[134:135], v[142:143], v[142:143]\n" \
"v_pk_mul_f32 v[136:137], v[140:141], v[212:213]\n" \
"v_pk_mul_f32 v[218:219], v[126:127], v[166:167]\n" \
"v_pk_fma_f32 v[220:221], v[154:155], v[154:155], v[150:151] neg_lo:[1,0,0] neg_hi:[1,0,0]\n" \
"v_pk_fma_f32 v[222:223], v[186:187], v[158:159], v[170:171] neg_lo:[1,0,0] neg_hi:[1,0,0]\n" \
"v_pk_fma_f32 v[106:107], v[156:157], v[156:157], v[152:153] neg_lo:[1,0,0] neg_hi:[1,0,0]\n" \
"v_pk_fma_f32 v[108:109], v[156:157], v[158:159], v[172:173] neg_lo:[1,0,0] neg_hi:[1,0,0]\n" \
"v_pk_fma_f32 v[188:189], v[166:167], v[166:167], v[190:191] neg_lo:[1,0,0] neg_hi:[1,0,0]\n" \
"v_pk_fma_f32 v[110:111], v[146:147], v[212:213], v[192:193] neg_lo:[1,0,0] neg_hi:[1,0,0]\n" \
"v_pk_fma_f32 v[104:105], v[158:159], v[166:167], v[194:195] neg_lo:[1,0,0] neg_hi:[1,0,0]\n" \
"v_pk_mul_f32 v[112:113], v[196:197], v[154:155]\n" \
"v_pk_fma_f32 v[118:119], v[186:187], v[126:127], v[198:199]\n" \
"v_pk_mul_f32 v[120:121], v[200:201], v[156:157]\n" \
"v_pk_fma_f32 v[114:115], v[156:157], v[126:127], v[176:177]\n" \
"v_pk_mul_f32 v[124:125], v[134:135], v[166:167]\n" \
"v_pk_fma_f32 v[182:183], v[146:147], v[206:207], v[136:137]\n" \
"v_pk_fma_f32 v[130:131], v[158:159], v[142:143], v[218:219]\n" \
"v_cvt_pk_f16_f32 v122, v132, v133\n" \
"v_cvt_pk_f16_f32 v123, v144, v145\n" \
"v_cvt_pk_f16_f32 v178, v116, v117\n" \
"v_cvt_pk_f16_f32 v179, v154, v155\n" \
"v_cvt_pk_f16_f32 v184, v138, v139\n" \
"v_cvt_pk_f16_f32 v185, v156, v157\n" \
"v_cvt_pk_f16_f32 v202, v126, v127\n" \
"v_cvt_pk_f16_f32 v203, v158, v159\n" \
"v_cvt_pk_f16_f32 v208, v140, v141\n" \
"v_cvt_pk_f16_f32 v209, v146, v147\n" \
"v_cvt_pk_f16_f32 v204, v128, v129\n" \
"v_cvt_pk_f16_f32 v205, v174, v175\n" \
"v_cvt_pk_f16_f32 v210, v142, v143\n" \
"v_cvt_pk_f16_f32 v211, v166, v167\n" \
"v_pk_mul_f32 v[214:215], v[220:221], v[116:117]\n" \
"v_pk_mul_f32 v[216:217], v[188:189], v[142:143]\n" \
"v_pk_mul_f32 v[180:181], v[188:189], v[188:189]\n" \
"v_pk_mul_f32 v[150:151], v[220:221], v[154:155]\n" \
"v_pk_mul_f32 v[170:171], v[188:189], v[166:167]\n" \
"v_pk_add_f32 v[152:153], v[188:189], v[188:189]\n" \
"v_pk_fma_f32 v[172:173], v[112:113], v[154:155], v[214:215] neg_lo:[1,0,0] neg_hi:[1,0,0]\n" \
"v_pk_fma_f32 v[190:191], v[124:125], v[166:167], v[216:217] neg_lo:[1,0,0] neg_hi:[1,0,0]\n" \
"v_pk_fma_f32 v[192:193], v[124:125], v[124:125], v[180:181] neg_lo:[1,0,0] neg_hi:[1,0,0]\n" \
"v_pk_fma_f32 v[212:213], v[112:113], v[116:117], v[150:151]\n" \
"v_pk_fma_f32 v[194:195], v[124:125], v[142:143], v[170:171]\n" \
"v_pk_mul_f32 v[196:197], v[152:153], v[124:125]\n" \
"v_cvt_pk_f16_f32 v198, v220, v221\n" \
"v_cvt_pk_f16_f32 v199, v112, v113\n" \
"v_cvt_pk_f16_f32 v186, v222, v223\n" \
"v_cvt_pk_f16_f32 v187, v118, v119\n" \
"v_cvt_pk_f16_f32 v200, v106, v107\n" \
"v_cvt_pk_f16_f32 v201, v120, v121\n" \
"v_cvt_pk_f16_f32 v176, v108, v109\n" \
"v_cvt_pk_f16_f32 v177, v114, v115\n" \
"v_cvt_pk_f16_f32 v134, v110, v111\n" \
"v_cvt_pk_f16_f32 v135, v182, v183\n" \
"v_cvt_pk_f16_f32 v206, v104, v105\n" \
"v_cvt_pk_f16_f32 v207, v130, v131\n" \
"v_cvt_pk_f16_f32 v136, v188, v189\n" \
"v_cvt_pk_f16_f32 v137, v124, v125\n" \
"v_cvt_pk_f16_f32 v218, v172, v173\n" \
"v_cvt_pk_f16_f32 v219, v212, v213\n" \
"v_cvt_pk_f16_f32 v132, v190, v191\n" \
"v_cvt_pk_f16_f32 v133, v194, v195\n" \
"v_cvt_pk_f16_f32 v144, v192, v193\n" \
"v_cvt_pk_f16_f32 v145, v196, v197\n" \
"v_mov_b32 v138, 0x3c003c00\n" \
"v_mov_b32 v139, 0\n" \
"ds_write_b64 v72, v[138:139] offset:0\n" \
"v_pk_mul_f16 v156, v160, v178\n" \
"v_pk_mul_f16 v126, v162, v178\n" \
"v_pk_mul_f16 v157, v160, v179\n" \
"v_pk_mul_f16 v127, v162, v179\n" \
"v_pk_fma_f16 v156, v161, v179, v156 neg_lo:[1,0,0] neg_hi:[1,0,0]\n" \
"v_pk_fma_f16 v126, v163, v179, v126 neg_lo:[1,0,0] neg_hi:[1,0,0]\n" \
"v_pk_fma_f16 v157, v161, v178, v157\n" \
"v_pk_fma_f16 v127, v163, v178, v127\n" \
"ds_write_b64 v72, v[156:157] offset:2640\n" \
"ds_write_b64 v72, v[126:127] offset:3168\n" \
"ds_write_b64 v72, v[160:161] offset:528\n" \
"v_pk_mul_f16 v158, v122, v178\n" \
"v_pk_mul_f16 v140, v160, v198\n" \
"v_pk_mul_f16 v159, v122, v179\n" \
"v_pk_mul_f16 v141, v160, v199\n" \
"v_pk_fma_f16 v158, v123, v179, v158 neg_lo:[1,0,0] neg_hi:[1,0,0]\n" \
"v_pk_fma_f16 v140, v161, v199, v140 neg_lo:[1,0,0] neg_hi:[1,0,0]\n" \
"v_pk_fma_f16 v159, v123, v178, v159\n" \
"v_pk_fma_f16 v141, v161, v198, v141\n" \
"ds_write_b64 v72, v[158:159] offset:3696\n" \
"ds_write_b64 v72, v[140:141] offset:4752\n" \
"ds_write_b64 v72, v[162:163] offset:1056\n" \
"v_pk_mul_f16 v146, v162, v198\n" \
"v_pk_mul_f16 v128, v122, v198\n" \
"v_pk_mul_f16 v147, v162, v199\n" \
"v_pk_mul_f16 v129, v122, v199\n" \
"v_pk_fma_f16 v146, v163, v199, v146 neg_lo:[1,0,0] neg_hi:[1,0,0]\n" \
"v_pk_fma_f16 v128, v123, v199, v128 neg_lo:[1,0,0] neg_hi:[1,0,0]\n" \
"v_pk_fma_f16 v147, v163, v198, v147\n" \
"v_pk_fma_f16 v129, v123, v198, v129\n" \
"ds_write_b64 v72, v[146:147] offset:5280\n" \
"ds_write_b64 v72, v[128:129] offset:5808\n" \
"ds_write_b64 v72, v[122:123] offset:1584\n" \
"v_pk_mul_f16 v174, v160, v218\n" \
"v_pk_mul_f16 v154, v162, v218\n" \
"v_pk_mul_f16 v175, v160, v219\n" \
"v_pk_mul_f16 v155, v162, v219\n" \
"v_pk_fma_f16 v174, v161, v219, v174 neg_lo:[1,0,0] neg_hi:[1,0,0]\n" \
"v_pk_fma_f16 v154, v163, v219, v154 neg_lo:[1,0,0] neg_hi:[1,0,0]\n" \
"v_pk_fma_f16 v175, v161, v218, v175\n" \
"v_pk_fma_f16 v155, v163, v218, v155\n" \
"ds_write_b64 v72, v[174:175] offset:6864\n" \
"ds_write_b64 v72, v[154:155] offset:7392\n" \
"ds_write_b64 v72, v[178:179] offset:2112\n" \
"v_pk_mul_f16 v214, v122, v218\n" \
"v_pk_mul_f16 v215, v122, v219\n" \
"v_pk_fma_f16 v214, v123, v219, v214 neg_lo:[1,0,0] neg_hi:[1,0,0]\n" \
"v_pk_fma_f16 v215, v123, v218, v215\n" \
"ds_write_b64 v72, v[214:215] offset:7920\n" \
"ds_write_b64 v72, v[198:199] offset:4224\n" \
"ds_write_b64 v72, v[218:219] offset:6336\n" \
"ds_read_b128 a[144:147], v73 offset:0\n" \
"ds_read_b128 a[148:151], v73 offset:64\n" \
"ds_read_b128 a[152:155], v73 offset:128\n" \
"ds_read_b128 a[156:159], v73 offset:192\n" \
"ds_read_b128 a[160:163], v73 offset:256\n" \
"ds_read_b128 a[164:167], v73 offset:320\n" \
"ds_read_b128 a[168:171], v73 offset:384\n" \
"ds_read_b128 a[172:175], v73 offset:448\n" \
"ds_write_b64 v72, v[148:149] offset:0\n" \
"v_pk_mul_f16 v166, v164, v148\n" \
"v_pk_mul_f16 v216, v164, v208\n" \
"v_pk_mul_f16 v167, v164, v149\n" \
"v_pk_mul_f16 v217, v164, v209\n" \
"v_pk_fma_f16 v166, v165, v149, v166 neg_lo:[1,0,0] neg_hi:[1,0,0]\n" \
"v_pk_fma_f16 v216, v165, v209, v216 neg_lo:[1,0,0] neg_hi:[1,0,0]\n" \
"v_pk_fma_f16 v167, v165, v148, v167\n" \
"v_pk_fma_f16 v217, v165, v208, v217\n" \
"ds_write_b64 v72, v[166:167] offset:528\n" \
"ds_write_b64 v72, v[216:217] offset:4752\n" \
"ds_write_b64 v72, v[208:209] offset:4224\n" \
"v_pk_mul_f16 v180, v168, v148\n" \
"v_pk_mul_f16 v116, v168, v208\n" \
"v_pk_mul_f16 v181, v168, v149\n" \
"v_pk_mul_f16 v117, v168, v209\n" \
"v_pk_fma_f16 v180, v169, v149, v180 neg_lo:[1,0,0] neg_hi:[1,0,0]\n" \
"v_pk_fma_f16 v116, v169, v209, v116 neg_lo:[1,0,0] neg_hi:[1,0,0]\n" \
"v_pk_fma_f16 v181, v169, v148, v181\n" \
"v_pk_fma_f16 v117, v169, v208, v117\n" \
"ds_write_b64 v72, v[180:181] offset:1056\n" \
"ds_write_b64 v72, v[116:117] offset:5280\n" \
"v_pk_mul_f16 v150, v184, v148\n" \
"v_pk_mul_f16 v170, v184, v208\n" \
"v_pk_mul_f16 v151, v184, v149\n" \
"v_pk_mul_f16 v171, v184, v209\n" \
"v_pk_fma_f16 v150, v185, v149, v150 neg_lo:[1,0,0] neg_hi:[1,0,0]\n" \
"v_pk_fma_f16 v170, v185, v209, v170 neg_lo:[1,0,0] neg_hi:[1,0,0]\n" \
"v_pk_fma_f16 v151, v185, v148, v151\n" \
"v_pk_fma_f16 v171, v185, v208, v171\n" \
"ds_write_b64 v72, v[150:151] offset:1584\n" \
"ds_write_b64 v72, v[170:171] offset:5808\n" \
"v_pk_mul_f16 v142, v202, v148\n" \
"v_pk_mul_f16 v152, v202, v208\n" \
"v_pk_mul_f16 v143, v202, v149\n" \
"v_pk_mul_f16 v153, v202, v209\n" \
"v_pk_fma_f16 v142, v203, v149, v142 neg_lo:[1,0,0] neg_hi:[1,0,0]\n" \
"v_pk_fma_f16 v152, v203, v209, v152 neg_lo:[1,0,0] neg_hi:[1,0,0]\n" \
"v_pk_fma_f16 v143, v203, v148, v143\n" \
"v_pk_fma_f16 v153, v203, v208, v153\n" \
"ds_write_b64 v72, v[142:143] offset:2112\n" \
"ds_write_b64 v72, v[152:153] offset:6336\n" \
"v_pk_mul_f16 v220, v186, v148\n" \
"v_pk_mul_f16 v112, v186, v208\n" \
"v_pk_mul_f16 v221, v186, v149\n" \
"v_pk_mul_f16 v113, v186, v209\n" \
"v_pk_fma_f16 v220, v187, v149, v220 neg_lo:[1,0,0] neg_hi:[1,0,0]\n" \
"v_pk_fma_f16 v112, v187, v209, v112 neg_lo:[1,0,0] neg_hi:[1,0,0]\n" \
"v_pk_fma_f16 v221, v187, v148, v221\n" \
"v_pk_fma_f16 v113, v187, v208, v113\n" \
"ds_write_b64 v72, v[220:221] offset:2640\n" \
"ds_write_b64 v72, v[112:113] offset:6864\n" \
"v_pk_mul_f16 v222, v200, v148\n" \
"v_pk_mul_f16 v118, v200, v208\n" \
"v_pk_mul_f16 v223, v200, v149\n" \
"v_pk_mul_f16 v119, v200, v209\n" \
"v_pk_fma_f16 v222, v201, v149, v222 neg_lo:[1,0,0] neg_hi:[1,0,0]\n" \
"v_pk_fma_f16 v118, v201, v209, v118 neg_lo:[1,0,0] neg_hi:[1,0,0]\n" \
"v_pk_fma_f16 v223, v201, v148, v223\n" \
"v_pk_fma_f16 v119, v201, v208, v119\n" \
"ds_write_b64 v72, v[222:223] offset:3168\n" \
"ds_write_b64 v72, v[118:119] offset:7392\n" \
"v_pk_mul_f16 v106, v176, v148\n" \
"v_pk_mul_f16 v120, v176, v208\n" \
"v_pk_mul_f16 v107, v176, v149\n" \
"v_pk_mul_f16 v121, v176, v209\n" \
"v_pk_fma_f16 v106, v177, v149, v106 neg_lo:[1,0,0] neg_hi:[1,0,0]\n" \
"v_pk_fma_f16 v120, v177, v209, v120 neg_lo:[1,0,0] neg_hi:[1,0,0]\n" \
"v_pk_fma_f16 v107, v177, v148, v107\n" \
"v_pk_fma_f16 v121, v177, v208, v121\n" \
"ds_write_b64 v72, v[106:107] offset:3696\n" \
"ds_write_b64 v72, v[120:121] offset:7920\n" \
"ds_read_b128 a[176:179], v73 offset:0\n" \
"ds_read_b128 a[180:183], v73 offset:64\n" \
"ds_read_b128 a[184:187], v73 offset:128\n" \
"ds_read_b128 a[188:191], v73 offset:192\n" \
"ds_read_b128 a[192:195], v73 offset:256\n" \
"ds_read_b128 a[196:199], v73 offset:320\n" \
"ds_read_b128 a[200:203], v73 offset:384\n" \
"ds_read_b128 a[204:207], v73 offset:448\n" \
"ds_write_b64 v72, v[204:205] offset:0\n" \
"v_pk_mul_f16 v108, v168, v204\n" \
"v_pk_mul_f16 v114, v168, v134\n" \
"v_pk_mul_f16 v109, v168, v205\n" \
"v_pk_mul_f16 v115, v168, v135\n" \
"v_pk_fma_f16 v108, v169, v205, v108 neg_lo:[1,0,0] neg_hi:[1,0,0]\n" \
"v_pk_fma_f16 v114, v169, v135, v114 neg_lo:[1,0,0] neg_hi:[1,0,0]\n" \
"v_pk_fma_f16 v109, v169, v204, v109\n" \
"v_pk_fma_f16 v115, v169, v134, v115\n" \
"ds_write_b64 v72, v[108:109] offset:528\n" \
"ds_write_b64 v72, v[114:115] offset:2640\n" \
"ds_write_b64 v72, v[134:135] offset:2112\n" \
"v_pk_mul_f16 v110, v202, v204\n" \
"v_pk_mul_f16 v182, v202, v134\n" \
"v_pk_mul_f16 v111, v202, v205\n" \
"v_pk_mul_f16 v183, v202, v135\n" \
"v_pk_fma_f16 v110, v203, v205, v110 neg_lo:[1,0,0] neg_hi:[1,0,0]\n" \
"v_pk_fma_f16 v182, v203, v135, v182 neg_lo:[1,0,0] neg_hi:[1,0,0]\n" \
"v_pk_fma_f16 v111, v203, v204, v111\n" \
"v_pk_fma_f16 v183, v203, v134, v183\n" \
"ds_write_b64 v72, v[110:111] offset:1056\n" \
"ds_write_b64 v72, v[182:183] offset:3168\n" \
"v_pk_mul_f16 v104, v200, v204\n" \
"v_pk_mul_f16 v130, v200, v134\n" \
"v_pk_mul_f16 v105, v200, v205\n" \
"v_pk_mul_f16 v131, v200, v135\n" \
"v_pk_fma_f16 v104, v201, v205, v104 neg_lo:[1,0,0] neg_hi:[1,0,0]\n" \
"v_pk_fma_f16 v130, v201, v135, v130 neg_lo:[1,0,0] neg_hi:[1,0,0]\n" \
"v_pk_fma_f16 v105, v201, v204, v105\n" \
"v_pk_fma_f16 v131, v201, v134, v131\n" \
"ds_write_b64 v72, v[104:105] offset:1584\n" \
"ds_write_b64 v72, v[130:131] offset:3696\n" \
"v_pk_mul_f16 v188, v148, v210\n" \
"v_pk_mul_f16 v124, v148, v206\n" \
"v_pk_mul_f16 v189, v148, v211\n" \
"v_pk_mul_f16 v125, v148, v207\n" \
"v_pk_fma_f16 v188, v149, v211, v188 neg_lo:[1,0,0] neg_hi:[1,0,0]\n" \
"v_pk_fma_f16 v124, v149, v207, v124 neg_lo:[1,0,0] neg_hi:[1,0,0]\n" \
"v_pk_fma_f16 v189, v149, v210, v189\n" \
"v_pk_fma_f16 v125, v149, v206, v125\n" \
"ds_write_b64 v72, v[188:189] offset:4224\n" \
"ds_write_b64 v72, v[124:125] offset:4752\n" \
"v_pk_mul_f16 v172, v208, v210\n" \
"v_pk_mul_f16 v212, v208, v206\n" \
"v_pk_mul_f16 v173, v208, v211\n" \
"v_pk_mul_f16 v213, v208, v207\n" \
"v_pk_fma_f16 v172, v209, v211, v172 neg_lo:[1,0,0] neg_hi:[1,0,0]\n" \
"v_pk_fma_f16 v212, v209, v207, v212 neg_lo:[1,0,0] neg_hi:[1,0,0]\n" \
"v_pk_fma_f16 v173, v209, v210, v173\n" \
"v_pk_fma_f16 v213, v209, v206, v213\n" \
"ds_write_b64 v72, v[172:173] offset:5280\n" \
"ds_write_b64 v72, v[212:213] offset:5808\n" \
"v_pk_mul_f16 v190, v204, v210\n" \
"v_pk_mul_f16 v194, v204, v206\n" \
"v_pk_mul_f16 v191, v204, v211\n" \
"v_pk_mul_f16 v195, v204, v207\n" \
"v_pk_fma_f16 v190, v205, v211, v190 neg_lo:[1,0,0] neg_hi:[1,0,0]\n" \
"v_pk_fma_f16 v194, v205, v207, v194 neg_lo:[1,0,0] neg_hi:[1,0,0]\n" \
"v_pk_fma_f16 v191, v205, v210, v191\n" \
"v_pk_fma_f16 v195, v205, v206, v195\n" \
"ds_write_b64 v72, v[190:191] offset:6336\n" \
"ds_write_b64 v72, v[194:195] offset:6864\n" \
"v_pk_mul_f16 v192, v134, v210\n" \
"v_pk_mul_f16 v196, v134, v206\n" \
"v_pk_mul_f16 v193, v134, v211\n" \
"v_pk_mul_f16 v197, v134, v207\n" \
"v_pk_fma_f16 v192, v135, v211, v192 neg_lo:[1,0,0] neg_hi:[1,0,0]\n" \
"v_pk_fma_f16 v196, v135, v207, v196 neg_lo:[1,0,0] neg_hi:[1,0,0]\n" \
"v_pk_fma_f16 v193, v135, v210, v193\n" \
"v_pk_fma_f16 v197, v135, v206, v197\n" \
"ds_write_b64 v72, v[192:193] offset:7392\n" \
"ds_write_b64 v72, v[196:197] offset:7920\n" \
"ds_read_b128 a[208:211], v73 offset:0\n" \
"ds_read_b128 a[212:215], v73 offset:64\n" \
"ds_read_b128 a[216:219], v73 offset:128\n" \
"ds_read_b128 a[220:223], v73 offset:192\n" \
"ds_read_b128 a[224:227], v73 offset:256\n" \
"ds_read_b128 a[228:231], v73 offset:320\n" \
"ds_read_b128 a[232:235], v73 offset:384\n" \
"ds_read_b128 a[236:239], v73 offset:448\n" \
"s_waitcnt lgkmcnt(0)\n" \
"v_mfma_f32_16x16x32_f16 v[156:159], a[176:179], a[144:147], 0\n" \
"v_mfma_f32_16x16x32_f16 v[156:159], a[180:183], a[148:151], v[156:159]\n" \
"v_mfma_f32_16x16x32_f16 v[156:159], a[184:187], a[152:155], v[156:159]\n" \
"v_mfma_f32_16x16x32_f16 v[156:159], a[188:191], a[156:159], v[156:159]\n" \
"v_mfma_f32_16x16x32_f16 v[156:159], a[192:195], a[160:163], v[156:159]\n" \
"v_mfma_f32_16x16x32_f16 v[156:159], a[196:199], a[164:167], v[156:159]\n" \
"v_mfma_f32_16x16x32_f16 v[156:159], a[200:203], a[168:171], v[156:159]\n" \
"v_mfma_f32_16x16x32_f16 v[156:159], a[204:207], a[172:175], v[156:159]\n" \
"v_pk_mul_f16 v138, v148, v136\n" \
"v_pk_mul_f16 v126, v208, v136\n" \
"v_pk_mul_f16 v139, v148, v137\n" \
"v_pk_mul_f16 v127, v208, v137\n" \
"v_pk_fma_f16 v138, v149, v137, v138 neg_lo:[1,0,0] neg_hi:[1,0,0]\n" \
"v_pk_fma_f16 v126, v209, v137, v126 neg_lo:[1,0,0] neg_hi:[1,0,0]\n" \
"v_pk_fma_f16 v139, v149, v136, v139\n" \
"v_pk_fma_f16 v127, v209, v136, v127\n" \
"ds_write_b64 v72, v[138:139] offset:0\n" \
"ds_write_b64 v72, v[126:127] offset:528\n" \
"v_pk_mul_f16 v140, v204, v136\n" \
"v_pk_mul_f16 v146, v134, v136\n" \
"v_pk_mul_f16 v141, v204, v137\n" \
"v_pk_mul_f16 v147, v134, v137\n" \
"v_pk_fma_f16 v140, v205, v137, v140 neg_lo:[1,0,0] neg_hi:[1,0,0]\n" \
"v_pk_fma_f16 v146, v135, v137, v146 neg_lo:[1,0,0] neg_hi:[1,0,0]\n" \
"v_pk_fma_f16 v141, v205, v136, v141\n" \
"v_pk_fma_f16 v147, v135, v136, v147\n" \
"ds_write_b64 v72, v[140:141] offset:1056\n" \
"ds_write_b64 v72, v[146:147] offset:1584\n" \
"v_pk_mul_f16 v128, v148, v132\n" \
"v_pk_mul_f16 v160, v208, v132\n" \
"v_pk_mul_f16 v129, v148, v133\n" \
"v_pk_mul_f16 v161, v208, v133\n" \
"v_pk_fma_f16 v128, v149, v133, v128 neg_lo:[1,0,0] neg_hi:[1,0,0]\n" \
"v_pk_fma_f16 v160, v209, v133, v160 neg_lo:[1,0,0] neg_hi:[1,0,0]\n" \
"v_pk_fma_f16 v129, v149, v132, v129\n" \
"v_pk_fma_f16 v161, v209, v132, v161\n" \
"ds_write_b64 v72, v[128:129] offset:2112\n" \
"ds_write_b64 v72, v[160:161] offset:2640\n" \
"v_pk_mul_f16 v162, v204, v132\n" \
"v_pk_mul_f16 v174, v134, v132\n" \
"v_pk_mul_f16 v163, v204, v133\n" \
"v_pk_mul_f16 v175, v134, v133\n" \
"v_pk_fma_f16 v162, v205, v133, v162 neg_lo:[1,0,0] neg_hi:[1,0,0]\n" \
"v_pk_fma_f16 v174, v135, v133, v174 neg_lo:[1,0,0] neg_hi:[1,0,0]\n" \
"v_pk_fma_f16 v163, v205, v132, v163\n" \
"v_pk_fma_f16 v175, v135, v132, v175\n" \
"ds_write_b64 v72, v[162:163] offset:3168\n" \
"ds_write_b64 v72, v[174:175] offset:3696\n" \
"v_pk_mul_f16 v154, v148, v144\n" \
"v_pk_mul_f16 v155, v148, v145\n" \
"v_pk_fma_f16 v154, v149, v145, v154 neg_lo:[1,0,0] neg_hi:[1,0,0]\n" \
"v_pk_fma_f16 v155, v149, v144, v155\n" \
"ds_write_b64 v72, v[154:155] offset:4224\n" \
"ds_read_b128 a[176:179], v73 offset:0\n" \
"ds_read_b128 a[180:183], v73 offset:64\n" \
"ds_read_b128 a[184:187], v73 offset:128\n" \
"ds_read_b128 a[188:191], v73 offset:192\n" \
"ds_read_b128 a[192:195], v73 offset:256\n" \
"ds_read_b128 a[196:199], v73 offset:320\n" \
"ds_read_b128 a[200:203], v73 offset:384\n" \
"ds_read_b128 a[204:207], v73 offset:448\n" \
"v_mfma_f32_16x16x32_f16 v[164:167], a[208:211], a[144:147], 0\n" \
"v_mfma_f32_16x16x32_f16 v[164:167], a[212:215], a[148:151], v[164:167]\n" \
"v_mfma_f32_16x16x32_f16 v[164:167], a[216:219], a[152:155], v[164:167]\n" \
"v_mfma_f32_16x16x32_f16 v[164:167], a[220:223], a[156:159], v[164:167]\n" \
"v_mfma_f32_16x16x32_f16 v[164:167], a[224:227], a[160:163], v[164:167]\n" \
"v_mfma_f32_16x16x32_f16 v[164:167], a[228:231], a[164:167], v[164:167]\n" \
"v_mfma_f32_16x16x32_f16 v[164:167], a[232:235], a[168:171], v[164:167]\n" \
"v_mfma_f32_16x16x32_f16 v[164:167], a[236:239], a[172:175], v[164:167]\n" \
"v_lshlrev_b32 v3, 2, v0\n" \
"v_add_u32 v3, 128, v3\n" \
"global_load_dword v4, v3, s[52:53] sc1\n" \
"s_waitcnt lgkmcnt(0)\n" \
"v_mfma_f32_16x16x32_f16 v[216:219], a[176:179], a[144:147], 0\n" \
"v_mfma_f32_16x16x32_f16 v[216:219], a[180:183], a[148:151], v[216:219]\n" \
"v_mfma_f32_16x16x32_f16 v[216:219], a[184:187], a[152:155], v[216:219]\n" \
"v_mfma_f32_16x16x32_f16 v[216:219], a[188:191], a[156:159], v[216:219]\n" \
"v_mfma_f32_16x16x32_f16 v[216:219], a[192:195], a[160:163], v[216:219]\n" \
"v_mfma_f32_16x16x32_f16 v[216:219], a[196:199], a[164:167], v[216:219]\n" \
"v_mfma_f32_16x16x32_f16 v[216:219], a[200:203], a[168:171], v[216:219]\n" \
"v_mfma_f32_16x16x32_f16 v[216:219], a[204:207], a[172:175], v[216:219]\n" \
"v_add_u32 v178, s88, v48\n" \
"v_add_u32 v179, s88, v49\n" \
"v_add_u32 v122, s88, v50\n" \
"v_add_u32 v123, s88, v51\n" \
"v_fma_f32 v214, v156, s76, v247\n" \
"v_mul_f32 v215, s76, v157\n" \
"v_cvt_pk_bf16_f32 v214, v214, v215\n" \
"v_mul_f32 v198, s76, v158\n" \
"v_mul_f32 v199, s76, v159\n" \
"v_cvt_pk_bf16_f32 v198, v198, v199\n" \
"v_mul_f32 v180, s76, v164\n" \
"v_mul_f32 v181, s76, v165\n" \
"v_cvt_pk_bf16_f32 v180, v180, v181\n" \
"v_mul_f32 v116, s76, v166\n" \
"v_mul_f32 v117, s76, v167\n" \
"v_cvt_pk_bf16_f32 v116, v116, v117\n" \
"ds_write_b32 v178, v214\n" \
"ds_write_b32 v178, v198 offset:4\n" \
"ds_write_b32 v179, v180\n" \
"ds_write_b32 v122, v116\n" \
"s_nop 1\n" \
"v_mul_f32 v184, s76, v216\n" \
"v_mul_f32 v185, s76, v217\n" \
"v_cvt_pk_bf16_f32 v184, v184, v185\n" \
"v_mul_f32 v150, s76, v218\n" \
"v_mul_f32 v151, s76, v219\n" \
"v_cvt_pk_bf16_f32 v150, v150, v151\n" \
"ds_write_b16 v123, v184\n" \
"ds_write_b16_d16_hi v123, v184 offset:32\n" \
"ds_write_b16 v123, v150 offset:64\n" \
"ds_write_b16_d16_hi v123, v150 offset:96\n" \
"s_setpc_b64 s[64:65]\n" \
"Lex_%=:\n" \
"v_add_u32 v104, s88, v52\n" \
"v_add_u32 v105, s88, v53\n" \
"v_add_u32 v106, s88, v54\n" \
"v_add_u32 v107, s88, v55\n" \
"v_add_u32 v108, s88, v56\n" \
"v_add_u32 v109, s88, v58\n" \
"v_add_u32 v110, s88, v59\n" \
"v_add_u32 v111, s88, v60\n" \
"v_add_u32 v112, s88, v61\n" \
"v_add_u32 v113, s88, v62\n" \
"v_add_u32 v114, s88, v63\n" \
"v_add_u32 v115, s88, v64\n" \
"v_add_u32 v116, s88, v65\n" \
"v_add_u32 v117, s88, v66\n" \
"v_add_u32 v118, s88, v67\n" \
"v_add_u32 v119, s88, v68\n" \
"v_add_u32 v120, s67, v74\n" \
"ds_read_u16 v124, v104 offset:2\n" \
"ds_read_u16 v125, v104\n" \
"ds_read_u16 v122, v104 offset:130\n" \
"ds_read_u16 v123, v104 offset:128\n" \
"ds_read_u16 v126, v106\n" \
"ds_read_u16 v127, v105\n" \
"ds_read_u16 v128, v105 offset:2\n" \
"ds_read_u16 v129, v105 offset:4\n" \
"ds_read_u16 v130, v107\n" \
"ds_read_u16 v131, v108\n" \
"ds_read_u16 v212, v109\n" \
"ds_read_u16 v213, v110\n" \
"ds_read_u16 v214, v111\n" \
"ds_read_u16 v215, v112\n" \
"ds_read_u16 v216, v113\n" \
"ds_read_u16 v217, v114\n" \
"ds_read_u16 v218, v115\n" \
"ds_read_u16 v219, v116\n" \
"ds_read_u16 v220, v117\n" \
"ds_read_u16 v221, v118\n" \
"ds_read_u16 v222, v119\n" \
"v_mov_b32 v140, 0\n" \
"v_mov_b32 v141, 0\n" \
"v_mov_b32 v142, 0\n" \
"v_mov_b32 v143, 0\n" \
"v_lshrrev_b32 v144, 1, v0\n" \
"v_mul_u32_u24 v144, 4128, v144\n" \
"v_add_u32 v144, v144, v120\n" \
"s_mov_b64 exec, 0xff\n" \
"ds_write_b128 v144, v[140:143] offset:4096\n" \
"s_mov_b64 exec, -1\n" \
"s_waitcnt lgkmcnt(15)\n" \
"v_perm_b32 v16, v122, v122, s86\n" \
"v_perm_b32 v17, v123, v122, v57\n" \
"v_perm_b32 v18, v123, v123, s86\n" \
"v_mov_b32 v148, v16\n" \
"v_mov_b32 v149, v17\n" \
"v_mov_b32 v150, v18\n" \
"v_mov_b32 v151, v18\n" \
"ds_write_b128 v120, v[148:151] offset:0\n" \
"v_alignbit_b32 v152, v17, v16, 16\n" \
"v_alignbit_b32 v153, v18, v17, 16\n" \
"v_alignbit_b32 v154, v18, v18, 16\n" \
"v_alignbit_b32 v155, v18, v18, 16\n" \
"ds_write_b128 v120, v[152:155] offset:4160\n" \
"v_mov_b32 v156, v17\n" \
"v_mov_b32 v157, v18\n" \
"v_mov_b32 v158, v18\n" \
"v_mov_b32 v159, v18\n" \
"ds_write_b128 v120, v[156:159] offset:8320\n" \
"v_alignbit_b32 v160, v18, v17, 16\n" \
"v_alignbit_b32 v161, v18, v18, 16\n" \
"v_alignbit_b32 v162, v18, v18, 16\n" \
"v_alignbit_b32 v163, v123, v18, 16\n" \
"ds_write_b128 v120, v[160:163] offset:12480\n" \
"v_perm_b32 v19, v124, v124, s86\n" \
"v_perm_b32 v20, v125, v124, v57\n" \
"v_perm_b32 v21, v125, v125, s86\n" \
"v_mov_b32 v164, v19\n" \
"v_mov_b32 v165, v20\n" \
"v_mov_b32 v166, v21\n" \
"v_mov_b32 v167, v21\n" \
"ds_write_b128 v120, v[164:167] offset:1024\n" \
"v_alignbit_b32 v168, v20, v19, 16\n" \
"v_alignbit_b32 v169, v21, v20, 16\n" \
"v_alignbit_b32 v170, v21, v21, 16\n" \
"v_alignbit_b32 v171, v21, v21, 16\n" \
"ds_write_b128 v120, v[168:171] offset:5184\n" \
"v_mov_b32 v172, v20\n" \
"v_mov_b32 v173, v21\n" \
"v_mov_b32 v174, v21\n" \
"v_mov_b32 v175, v21\n" \
"ds_write_b128 v120, v[172:175] offset:9344\n" \
"v_alignbit_b32 v176, v21, v20, 16\n" \
"v_alignbit_b32 v177, v21, v21, 16\n" \
"v_alignbit_b32 v178, v21, v21, 16\n" \
"v_alignbit_b32 v179, v126, v21, 16\n" \
"ds_write_b128 v120, v[176:179] offset:13504\n" \
"s_waitcnt lgkmcnt(8)\n" \
"v_perm_b32 v22, v129, v129, s86\n" \
"v_perm_b32 v23, v128, v128, s86\n" \
"v_perm_b32 v24, v127, v128, s86\n" \
"v_perm_b32 v25, v127, v127, s86\n" \
"v_perm_b32 v26, v130, v127, s86\n" \
"v_mov_b32 v180, v22\n" \
"v_mov_b32 v181, v23\n" \
"v_mov_b32 v182, v24\n" \
"v_mov_b32 v183, v25\n" \
"ds_write_b128 v120, v[180:183] offset:2048\n" \
"v_alignbit_b32 v184, v23, v22, 16\n" \
"v_alignbit_b32 v185, v24, v23, 16\n" \
"v_alignbit_b32 v186, v25, v24, 16\n" \
"v_alignbit_b32 v187, v26, v25, 16\n" \
"ds_write_b128 v120, v[184:187] offset:6208\n" \
"v_mov_b32 v188, v23\n" \
"v_mov_b32 v189, v24\n" \
"v_mov_b32 v190, v25\n" \
"v_mov_b32 v191, v26\n" \
"ds_write_b128 v120, v[188:191] offset:10368\n" \
"v_alignbit_b32 v192, v24, v23, 16\n" \
"v_alignbit_b32 v193, v25, v24, 16\n" \
"v_alignbit_b32 v194, v26, v25, 16\n" \
"v_alignbit_b32 v195, v131, v26, 16\n" \
"ds_write_b128 v120, v[192:195] offset:14528\n" \
"v_perm_b32 v132, v213, v212, s86\n" \
"v_perm_b32 v133, v215, v214, s86\n" \
"v_perm_b32 v134, v217, v216, s86\n" \
"v_perm_b32 v135, v219, v218, s86\n" \
"v_perm_b32 v136, v221, v220, s86\n" \
"v_mov_b32 v196, v132\n" \
"v_mov_b32 v197, v133\n" \
"v_mov_b32 v198, v134\n" \
"v_mov_b32 v199, v135\n" \
"ds_write_b128 v120, v[196:199] offset:3072\n" \
"v_alignbit_b32 v200, v133, v132, 16\n" \
"v_alignbit_b32 v201, v134, v133, 16\n" \
"v_alignbit_b32 v202, v135, v134, 16\n" \
"v_alignbit_b32 v203, v136, v135, 16\n" \
"ds_write_b128 v120, v[200:203] offset:7232\n" \
"v_mov_b32 v204, v133\n" \
"v_mov_b32 v205, v134\n" \
"v_mov_b32 v206, v135\n" \
"v_mov_b32 v207, v136\n" \
"ds_write_b128 v120, v[204:207] offset:11392\n" \
"v_alignbit_b32 v208, v134, v133, 16\n" \
"v_alignbit_b32 v209, v135, v134, 16\n" \
"v_alignbit_b32 v210, v136, v135, 16\n" \
"v_alignbit_b32 v211, v222, v136, 16\n" \
"ds_write_b128 v120, v[208:211] offset:15552\n" \
"s_setpc_b64 s[64:65]\n" \
"Lcl_%=:\n" \
"ds_read_b128 a[32:35], v2 offset:64\n" \
"ds_read_b64 a[0:1], v1 offset:3648\n" \
"ds_read_b64 a[2:3], v1 offset:3656\n" \
"ds_read_b128 a[36:39], v2 offset:576\n" \
"ds_read_b64 a[4:5], v1 offset:3136\n" \
"ds_read_b64 a[6:7], v1 offset:3144\n" \
"ds_read_b128 a[40:43], v2 offset:1088\n" \
"ds_read_b64 a[8:9], v1 offset:2624\n" \
"ds_read_b64 a[10:11], v1 offset:2632\n" \
"ds_read_b128 a[44:47], v2 offset:1600\n" \
"ds_read_b64 a[12:13], v1 offset:2112\n" \
"ds_read_b64 a[14:15], v1 offset:2120\n" \
"ds_read_b128 a[48:51], v2 offset:2112\n" \
"ds_read_b64 a[16:17], v1 offset:1600\n" \
"ds_read_b64 a[18:19], v1 offset:1608\n" \
"ds_read_b128 a[52:55], v2 offset:2624\n" \
"ds_read_b64 a[20:21], v1 offset:1088\n" \
"ds_read_b64 a[22:23], v1 offset:1096\n" \
"ds_read_b128 a[56:59], v2 offset:3136\n" \
"ds_read_b64 a[24:25], v1 offset:576\n" \
"ds_read_b64 a[26:27], v1 offset:584\n" \
"ds_read_b128 a[60:63], v2 offset:3648\n" \
"ds_read_b64 a[28:29], v1 offset:64\n" \
"ds_read_b64 a[30:31], v1 offset:72\n" \
"v_mov_b32 v16, 0\n" \
"v_mov_b32 v17, 0\n" \
"v_mov_b32 v18, 0\n" \
"v_mov_b32 v19, 0\n" \
"v_mov_b32 v20, 0\n" \
"v_mov_b32 v21, 0\n" \
"v_mov_b32 v22, 0\n" \
"v_mov_b32 v23, 0\n" \
"v_mov_b32 v24, 0\n" \
"v_mov_b32 v25, 0\n" \
"v_mov_b32 v26, 0\n" \
"v_mov_b32 v27, 0\n" \
"v_mov_b32 v28, 0\n" \
"v_mov_b32 v29, 0\n" \
"v_mov_b32 v30, 0\n" \
"v_mov_b32 v31, 0\n" \
"v_mov_b32 v32, 0\n" \
"v_mov_b32 v33, 0\n" \
"v_mov_b32 v34, 0\n" \
"v_mov_b32 v35, 0\n" \
"v_mov_b32 v36, 0\n" \
"v_mov_b32 v37, 0\n" \
"v_mov_b32 v38, 0\n" \
"v_mov_b32 v39, 0\n" \
"v_mov_b32 v40, 0\n" \
"v_mov_b32 v41, 0\n" \
"v_mov_b32 v42, 0\n" \
"v_mov_b32 v43, 0\n" \
"v_mov_b32 v44, 0\n" \
"v_mov_b32 v45, 0\n" \
"v_mov_b32 v46, 0\n" \
"v_mov_b32 v47, 0\n" \
"Lcv_%=:\n" \
"s_waitcnt lgkmcnt(0)\n" \
"v_mfma_f32_16x16x32_bf16 v[16:19], a[0:3], a[32:35], v[16:19]\n" \
"ds_read_b128 a[96:99], v2 offset:0\n" \
"v_mfma_f32_16x16x32_bf16 v[20:23], a[0:3], a[36:39], v[20:23]\n" \
"ds_read_b64 a[64:65], v1 offset:3584\n" \
"v_mfma_f32_16x16x32_bf16 v[24:27], a[0:3], a[40:43], v[24:27]\n" \
"ds_read_b64 a[66:67], v1 offset:3592\n" \
"v_mfma_f32_16x16x32_bf16 v[28:31], a[0:3], a[44:47], v[28:31]\n" \
"ds_read_b128 a[100:103], v2 offset:512\n" \
"v_mfma_f32_16x16x32_bf16 v[32:35], a[0:3], a[48:51], v[32:35]\n" \
"ds_read_b64 a[68:69], v1 offset:3072\n" \
"v_mfma_f32_16x16x32_bf16 v[36:39], a[0:3], a[52:55], v[36:39]\n" \
"ds_read_b64 a[70:71], v1 offset:3080\n" \
"v_mfma_f32_16x16x32_bf16 v[40:43], a[0:3], a[56:59], v[40:43]\n" \
"ds_read_b128 a[104:107], v2 offset:1024\n" \
"v_mfma_f32_16x16x32_bf16 v[44:47], a[0:3], a[60:63], v[44:47]\n" \
"ds_read_b64 a[72:73], v1 offset:2560\n" \
"v_mfma_f32_16x16x32_bf16 v[20:23], a[4:7], a[32:35], v[20:23]\n" \
"ds_read_b64 a[74:75], v1 offset:2568\n" \
"v_mfma_f32_16x16x32_bf16 v[24:27], a[4:7], a[36:39], v[24:27]\n" \
"ds_read_b128 a[108:111], v2 offset:1536\n" \
"v_mfma_f32_16x16x32_bf16 v[28:31], a[4:7], a[40:43], v[28:31]\n" \
"ds_read_b64 a[76:77], v1 offset:2048\n" \
"v_mfma_f32_16x16x32_bf16 v[32:35], a[4:7], a[44:47], v[32:35]\n" \
"ds_read_b64 a[78:79], v1 offset:2056\n" \
"v_mfma_f32_16x16x32_bf16 v[36:39], a[4:7], a[48:51], v[36:39]\n" \
"ds_read_b128 a[112:115], v2 offset:2048\n" \
"v_mfma_f32_16x16x32_bf16 v[40:43], a[4:7], a[52:55], v[40:43]\n" \
"ds_read_b64 a[80:81], v1 offset:1536\n" \
"v_mfma_f32_16x16x32_bf16 v[44:47], a[4:7], a[56:59], v[44:47]\n" \
"ds_read_b64 a[82:83], v1 offset:1544\n" \
"v_mfma_f32_16x16x32_bf16 v[24:27], a[8:11], a[32:35], v[24:27]\n" \
"ds_read_b128 a[116:119], v2 offset:2560\n" \
"v_mfma_f32_16x16x32_bf16 v[28:31], a[8:11], a[36:39], v[28:31]\n" \
"ds_read_b64 a[84:85], v1 offset:1024\n" \
"v_mfma_f32_16x16x32_bf16 v[32:35], a[8:11], a[40:43], v[32:35]\n" \
"ds_read_b64 a[86:87], v1 offset:1032\n" \
"v_mfma_f32_16x16x32_bf16 v[36:39], a[8:11], a[44:47], v[36:39]\n" \
"ds_read_b128 a[120:123], v2 offset:3072\n" \
"v_mfma_f32_16x16x32_bf16 v[40:43], a[8:11], a[48:51], v[40:43]\n" \
"ds_read_b64 a[88:89], v1 offset:512\n" \
"v_mfma_f32_16x16x32_bf16 v[44:47], a[8:11], a[52:55], v[44:47]\n" \
"ds_read_b64 a[90:91], v1 offset:520\n" \
"v_mfma_f32_16x16x32_bf16 v[28:31], a[12:15], a[32:35], v[28:31]\n" \
"ds_read_b128 a[124:127], v2 offset:3584\n" \
"v_mfma_f32_16x16x32_bf16 v[32:35], a[12:15], a[36:39], v[32:35]\n" \
"ds_read_b64 a[92:93], v1 offset:0\n" \
"v_mfma_f32_16x16x32_bf16 v[36:39], a[12:15], a[40:43], v[36:39]\n" \
"ds_read_b64 a[94:95], v1 offset:8\n" \
"v_mfma_f32_16x16x32_bf16 v[40:43], a[12:15], a[44:47], v[40:43]\n" \
"v_mfma_f32_16x16x32_bf16 v[44:47], a[12:15], a[48:51], v[44:47]\n" \
"v_mfma_f32_16x16x32_bf16 v[32:35], a[16:19], a[32:35], v[32:35]\n" \
"v_mfma_f32_16x16x32_bf16 v[36:39], a[16:19], a[36:39], v[36:39]\n" \
"v_mfma_f32_16x16x32_bf16 v[40:43], a[16:19], a[40:43], v[40:43]\n" \
"v_mfma_f32_16x16x32_bf16 v[44:47], a[16:19], a[44:47], v[44:47]\n" \
"v_mfma_f32_16x16x32_bf16 v[36:39], a[20:23], a[32:35], v[36:39]\n" \
"v_mfma_f32_16x16x32_bf16 v[40:43], a[20:23], a[36:39], v[40:43]\n" \
"v_mfma_f32_16x16x32_bf16 v[44:47], a[20:23], a[40:43], v[44:47]\n" \
"v_mfma_f32_16x16x32_bf16 v[40:43], a[24:27], a[32:35], v[40:43]\n" \
"v_mfma_f32_16x16x32_bf16 v[44:47], a[24:27], a[36:39], v[44:47]\n" \
"v_mfma_f32_16x16x32_bf16 v[44:47], a[28:31], a[32:35], v[44:47]\n" \
"s_waitcnt lgkmcnt(0)\n" \
"v_mfma_f32_16x16x32_bf16 v[16:19], a[64:67], a[96:99], v[16:19]\n" \
"v_add_u32 v1, -128, v1\n" \
"v_mfma_f32_16x16x32_bf16 v[20:23], a[64:67], a[100:103], v[20:23]\n" \
"v_add_u32 v2, -128, v2\n" \
"v_mfma_f32_16x16x32_bf16 v[24:27], a[64:67], a[104:107], v[24:27]\n" \
"ds_read_b128 a[32:35], v2 offset:64\n" \
"v_mfma_f32_16x16x32_bf16 v[28:31], a[64:67], a[108:111], v[28:31]\n" \
"ds_read_b64 a[0:1], v1 offset:3648\n" \
"v_mfma_f32_16x16x32_bf16 v[32:35], a[64:67], a[112:115], v[32:35]\n" \
"ds_read_b64 a[2:3], v1 offset:3656\n" \
"v_mfma_f32_16x16x32_bf16 v[36:39], a[64:67], a[116:119], v[36:39]\n" \
"ds_read_b128 a[36:39], v2 offset:576\n" \
"v_mfma_f32_16x16x32_bf16 v[40:43], a[64:67], a[120:123], v[40:43]\n" \
"ds_read_b64 a[4:5], v1 offset:3136\n" \
"v_mfma_f32_16x16x32_bf16 v[44:47], a[64:67], a[124:127], v[44:47]\n" \
"ds_read_b64 a[6:7], v1 offset:3144\n" \
"v_mfma_f32_16x16x32_bf16 v[20:23], a[68:71], a[96:99], v[20:23]\n" \
"ds_read_b128 a[40:43], v2 offset:1088\n" \
"v_mfma_f32_16x16x32_bf16 v[24:27], a[68:71], a[100:103], v[24:27]\n" \
"ds_read_b64 a[8:9], v1 offset:2624\n" \
"v_mfma_f32_16x16x32_bf16 v[28:31], a[68:71], a[104:107], v[28:31]\n" \
"ds_read_b64 a[10:11], v1 offset:2632\n" \
"v_mfma_f32_16x16x32_bf16 v[32:35], a[68:71], a[108:111], v[32:35]\n" \
"ds_read_b128 a[44:47], v2 offset:1600\n" \
"v_mfma_f32_16x16x32_bf16 v[36:39], a[68:71], a[112:115], v[36:39]\n" \
"ds_read_b64 a[12:13], v1 offset:2112\n" \
"v_mfma_f32_16x16x32_bf16 v[40:43], a[68:71], a[116:119], v[40:43]\n" \
"ds_read_b64 a[14:15], v1 offset:2120\n" \
"v_mfma_f32_16x16x32_bf16 v[44:47], a[68:71], a[120:123], v[44:47]\n" \
"ds_read_b128 a[48:51], v2 offset:2112\n" \
"v_mfma_f32_16x16x32_bf16 v[24:27], a[72:75], a[96:99], v[24:27]\n" \
"ds_read_b64 a[16:17], v1 offset:1600\n" \
"v_mfma_f32_16x16x32_bf16 v[28:31], a[72:75], a[100:103], v[28:31]\n" \
"ds_read_b64 a[18:19], v1 offset:1608\n" \
"v_mfma_f32_16x16x32_bf16 v[32:35], a[72:75], a[104:107], v[32:35]\n" \
"ds_read_b128 a[52:55], v2 offset:2624\n" \
"v_mfma_f32_16x16x32_bf16 v[36:39], a[72:75], a[108:111], v[36:39]\n" \
"ds_read_b64 a[20:21], v1 offset:1088\n" \
"v_mfma_f32_16x16x32_bf16 v[40:43], a[72:75], a[112:115], v[40:43]\n" \
"ds_read_b64 a[22:23], v1 offset:1096\n" \
"v_mfma_f32_16x16x32_bf16 v[44:47], a[72:75], a[116:119], v[44:47]\n" \
"ds_read_b128 a[56:59], v2 offset:3136\n" \
"v_mfma_f32_16x16x32_bf16 v[28:31], a[76:79], a[96:99], v[28:31]\n" \
"ds_read_b64 a[24:25], v1 offset:576\n" \
"v_mfma_f32_16x16x32_bf16 v[32:35], a[76:79], a[100:103], v[32:35]\n" \
"ds_read_b64 a[26:27], v1 offset:584\n" \
"v_mfma_f32_16x16x32_bf16 v[36:39], a[76:79], a[104:107], v[36:39]\n" \
"ds_read_b128 a[60:63], v2 offset:3648\n" \
"v_mfma_f32_16x16x32_bf16 v[40:43], a[76:79], a[108:111], v[40:43]\n" \
"ds_read_b64 a[28:29], v1 offset:64\n" \
"v_mfma_f32_16x16x32_bf16 v[44:47], a[76:79], a[112:115], v[44:47]\n" \
"ds_read_b64 a[30:31], v1 offset:72\n" \
"v_mfma_f32_16x16x32_bf16 v[32:35], a[80:83], a[96:99], v[32:35]\n" \
"v_mfma_f32_16x16x32_bf16 v[36:39], a[80:83], a[100:103], v[36:39]\n" \
"v_mfma_f32_16x16x32_bf16 v[40:43], a[80:83], a[104:107], v[40:43]\n" \
"v_mfma_f32_16x16x32_bf16 v[44:47], a[80:83], a[108:111], v[44:47]\n" \
"v_mfma_f32_16x16x32_bf16 v[36:39], a[84:87], a[96:99], v[36:39]\n" \
"v_mfma_f32_16x16x32_bf16 v[40:43], a[84:87], a[100:103], v[40:43]\n" \
"v_mfma_f32_16x16x32_bf16 v[44:47], a[84:87], a[104:107], v[44:47]\n" \
"v_mfma_f32_16x16x32_bf16 v[40:43], a[88:91], a[96:99], v[40:43]\n" \
"v_mfma_f32_16x16x32_bf16 v[44:47], a[88:91], a[100:103], v[44:47]\n" \
"v_mfma_f32_16x16x32_bf16 v[44:47], a[92:95], a[96:99], v[44:47]\n" \
"s_sub_u32 s77, s77, 1\n" \
"s_cmp_lg_u32 s77, 0\n" \
"s_cbranch_scc1 Lcv_%=\n" \
"s_waitcnt lgkmcnt(0)\n" \
"s_nop 4\n" \
"s_setpc_b64 s[64:65]\n" \
"Lst_%=:\n" \
"v_cvt_pk_bf16_f32 v104, v16, v17\n" \
"v_cvt_pk_bf16_f32 v105, v18, v19\n" \
"v_cvt_pk_bf16_f32 v106, v20, v21\n" \
"v_cvt_pk_bf16_f32 v107, v22, v23\n" \
"v_cvt_pk_bf16_f32 v108, v24, v25\n" \
"v_cvt_pk_bf16_f32 v109, v26, v27\n" \
"v_cvt_pk_bf16_f32 v110, v28, v29\n" \
"v_cvt_pk_bf16_f32 v111, v30, v31\n" \
"v_cvt_pk_bf16_f32 v112, v32, v33\n" \
"v_cvt_pk_bf16_f32 v113, v34, v35\n" \
"v_cvt_pk_bf16_f32 v114, v36, v37\n" \
"v_cvt_pk_bf16_f32 v115, v38, v39\n" \
"v_cvt_pk_bf16_f32 v116, v40, v41\n" \
"v_cvt_pk_bf16_f32 v117, v42, v43\n" \
"v_cvt_pk_bf16_f32 v118, v44, v45\n" \
"v_cvt_pk_bf16_f32 v119, v46, v47\n" \
"s_lshl_b32 s67, s87, 12\n" \
"v_permlane16_swap_b32 v104, v106\n" \
"v_permlane16_swap_b32 v105, v107\n" \
"v_permlane16_swap_b32 v108, v110\n" \
"v_permlane16_swap_b32 v109, v111\n" \
"v_permlane16_swap_b32 v112, v114\n" \
"v_permlane16_swap_b32 v113, v115\n" \
"v_permlane16_swap_b32 v116, v118\n" \
"v_permlane16_swap_b32 v117, v119\n" \
"buffer_store_dwordx4 v[104:107], v71, s[48:51], s67 offen offset:0 sc1\n" \
"buffer_store_dwordx4 v[108:111], v71, s[48:51], s67 offen offset:1024 sc1\n" \
"buffer_store_dwordx4 v[112:115], v71, s[48:51], s67 offen offset:2048 sc1\n" \
"buffer_store_dwordx4 v[116:119], v71, s[48:51], s67 offen offset:3072 sc1\n" \
"s_setpc_b64 s[64:65]\n" \
"Lend_%=:\n" \
"v_min_u32 v3, 11, v0\n" \
"v_lshlrev_b32 v3, 11, v3\n" \
"s_lshr_b32 s67, s56, 2\n" \
"s_mul_i32 s67, s67, 24576\n" \
"s_add_u32 s67, s67, 640\n" \
"v_add_u32 v3, s67, v3\n" \
"v_mov_b32 v6, 0\n" \
"v_mov_b32 v4, s54\n" \
"v_mov_b32 v5, s54\n" \
"s_mov_b64 exec, 0xfff\n" \
"global_atomic_add v5, v3, v6, s[52:53] offset:384 sc0 sc1\n" \
"global_atomic_add v4, v3, v6, s[52:53] offset:640 sc0 sc1\n" \
"s_mov_b64 exec, -1\n" \
"s_waitcnt vmcnt(0) lgkmcnt(0)\n" \
"v_cmp_eq_u32 vcc, s54, v4\n" \
"s_cmp_eq_u64 vcc, -1\n" \
"s_cselect_b32 s67, 2, 0\n" \
"v_cmp_eq_u32 vcc, s54, v5\n" \
"s_cmp_eq_u64 vcc, -1\n" \
"s_cselect_b32 s68, 1, 0\n" \
"s_or_b32 s67, s67, s68\n" \
""
typedef unsigned short u16;
typedef unsigned int u32;
typedef float f32x2 __attribute__((ext_vector_type(2)));
typedef u32 u32x2 __attribute__((ext_vector_type(2)));
typedef u32 u32x4 __attribute__((ext_vector_type(4)));
typedef short s16x4 __attribute__((ext_vector_type(4)));
typedef __bf16 bf16x2 __attribute__((ext_vector_type(2)));
#define LAS __attribute__((address_space(3)))
typedef u32 __attribute__((may_alias)) a_u32;
typedef u32x4 __attribute__((may_alias)) a_u32x4;
#define GAS __attribute__((address_space(1)))
#define DI __device__ __forceinline__

constexpr int D_ = 576;
constexpr size_t WS_CTL = 0;
constexpr int C_LN = 32, C_SD = C_LN + 64, C_FA = C_SD + 64, C_ACK = C_FA + 192 * 16 * 32;
constexpr size_t WS_RSTD = 397312;
constexpr size_t WS_XNT = 409600;
constexpr size_t WS_AT = WS_XNT + 2359296;
constexpr size_t WS_PART = WS_AT + 2359296;
constexpr int LDS_BYTES = 163840;

DI float bf_lo(u32 v) { return __uint_as_float(v << 16); }
DI float bf_hi(u32 v) { return __uint_as_float(v & 0xffff0000u); }
DI u32 pk_bf(float a, float b) { f32x2 v = {a, b}; return __builtin_bit_cast(u32, __builtin_convertvector(v, bf16x2)); }
DI float rbf(float a) { return bf_lo(pk_bf(a, a)); }
DI float dot2b(u32 a, u32 b, float c) { return __builtin_amdgcn_fdot2_f32_bf16(__builtin_bit_cast(bf16x2, a), __builtin_bit_cast(bf16x2, b), c, false); }
#define RLX_LD(p) __hip_atomic_load((p), __ATOMIC_RELAXED, __HIP_MEMORY_SCOPE_AGENT)
#define RLX_ST(p, v) __hip_atomic_store((p), (v), __ATOMIC_RELAXED, __HIP_MEMORY_SCOPE_AGENT)
#define MKRSRC(p, n) __builtin_amdgcn_make_buffer_rsrc((void*)(p), 0, (n), 0x00020000)

DI u32 flag_ld(GAS u32* p) {
  u32 r;
  asm volatile("global_atomic_add %0, %1, %2, off sc0 sc1\n s_waitcnt vmcnt(0)" : "=&v"(r) : "v"(p), "v"(0u) : "memory");
  return r;
}
template <int NAP> DI void poll_lv(GAS u32* p, u32 tag, int lane) {
  u32 spins = 0;
  while (true) {
    u32 f = tag;
    if (lane < 12) f = flag_ld(p);
    if (__all(f == tag) || ++spins > (1u << 22)) break;
    __builtin_amdgcn_s_sleep(NAP);
  }
  __builtin_amdgcn_fence(__ATOMIC_ACQUIRE, "wavefront");
}
template <int NAP> DI void poll_eq(GAS u32* p, u32 tag) {
  u32 f = RLX_LD(p), spins = 0;
  while (!__all(f == tag)) {
    if (++spins > (1u << 22)) break;
    __builtin_amdgcn_s_sleep(NAP);
    f = RLX_LD(p);
  }
  __builtin_amdgcn_fence(__ATOMIC_ACQUIRE, "wavefront");
}
DI void pf_line(__amdgpu_buffer_rsrc_t r, u32 voff, u32 soff) { asm volatile("s_nop 4\n buffer_load_dword a250, %0, %1, %2 offen sc1" :: "v"(voff), "s"(r), "s"(soff) : "a250"); }

struct Piece { u32x4 pv[12], av, xv; float rs; };
DI void piece_load_a(Piece& P, char* ws, int i, int hb, int lane) {
  const int col0 = (hb >> 3) * 32, row0 = 128 * i + (hb & 7) * 16;
  P.av = __builtin_amdgcn_raw_buffer_load_b128(MKRSRC(ws + WS_AT, 2359296), (lane >> 1) * 4096 + (lane & 1) * 16, col0 * 4096 + row0 * 2, 16);
}
DI void piece_load_x(Piece& P, const u16* x, char* ws, int i, int hb, int lane) {
  const int col0 = (hb >> 3) * 32, row0 = 128 * i + (hb & 7) * 16;
  P.xv = *(const u32x4*)(x + (size_t)(row0 + (lane & 15)) * D_ + col0 + (lane & 16) + 8 * (lane >> 5));
  P.rs = __builtin_bit_cast(float, __builtin_amdgcn_raw_buffer_load_b32(MKRSRC(ws + WS_RSTD, 8192), (lane & 15) * 4, row0 * 4, 16));
}
DI void piece_load_side(Piece& P, const u16* x, char* ws, int i, int hb, int lane) { piece_load_a(P, ws, i, hb, lane); piece_load_x(P, x, ws, i, hb, lane); }
DI void piece_load_part(Piece& P, char* ws, int i, int hb, int lane) {
  auto rp = MKRSRC(ws + WS_PART + (size_t)i * 18 * 4 * 12 * 2048, 18 * 4 * 12 * 2048);
  #pragma unroll
  for (int sp = 0; sp < 12; ++sp) P.pv[sp] = __builtin_amdgcn_raw_buffer_load_b128(rp, lane * 16, ((hb >> 1) * 12 + sp) * 2048 + (hb & 1) * 1024, 16);
}
DI void piece_finish(const Piece& P, u16* out, int i, int hb, int lane, LAS char* s3) {
  u32 clo = 0x00003f80u, chi = 0x3f800000u;
  asm volatile("" : "+s"(clo), "+s"(chi));
  const int h = lane >> 5;
  float f[8];
  #pragma unroll
  for (int e = 0; e < 4; ++e) { f[2 * e] = bf_lo(P.pv[0][e]); f[2 * e + 1] = bf_hi(P.pv[0][e]); }
  #pragma unroll
  for (int sp = 1; sp < 12; ++sp)
    #pragma unroll
    for (int e = 0; e < 4; ++e) { f[2 * e] = dot2b(P.pv[sp][e], clo, f[2 * e]); f[2 * e + 1] = dot2b(P.pv[sp][e], chi, f[2 * e + 1]); }
  *(LAS a_u32x4*)(s3 + lane * 16) = P.av;
  u32 F[4], A[4];
  #pragma unroll
  for (int e = 0; e < 4; ++e) F[e] = pk_bf(f[2 * e] * P.rs, f[2 * e + 1] * P.rs);
  #pragma unroll
  for (int q = 0; q < 2; ++q) {
    const u32x2 a2 = __builtin_bit_cast(u32x2, __builtin_amdgcn_ds_read_tr16_b64_v4i16((LAS s16x4*)(s3 + (16 * q + (lane >> 2)) * 32 + (lane & 3) * 8)));
    A[2 * q] = a2.x; A[2 * q + 1] = a2.y;
  }
  #pragma unroll
  for (int e = 0; e < 2; ++e) {
    auto r1 = __builtin_amdgcn_permlane16_swap(F[e], F[2 + e], false, false); F[e] = r1[0]; F[2 + e] = r1[1];
    auto r2 = __builtin_amdgcn_permlane16_swap(A[e], A[2 + e], false, false); A[e] = r2[0]; A[2 + e] = r2[1];
  }
  u32x4 o;
  #pragma unroll
  for (int e = 0; e < 4; ++e) {
    const u32 z = pk_bf(bf_lo(P.xv[e]) + bf_lo(A[e]), bf_hi(P.xv[e]) + bf_hi(A[e]));
    o[e] = pk_bf(bf_lo(z) + bf_lo(F[e]), bf_hi(z) + bf_hi(F[e]));
  }
  const int col0 = (hb >> 3) * 32, row0 = 128 * i + (hb & 7) * 16;
  __builtin_amdgcn_raw_buffer_store_b128(o, MKRSRC(out, 2359296), ((lane & 15) * D_ + (lane & 16) + 8 * h) * 2, (row0 * D_ + col0) * 2, 16);
  asm volatile("s_nop 1" :: "v"(o));
}

struct SsmArgs { const u16 *x, *are, *aim, *bre, *bim, *cre, *cim, *ldt, *dsk, *w1, *w2; char* ws; u16* out; };
struct ChP { u32 p[6]; u32 l; };
DI ChP load_ch(const SsmArgs& a, int ch, int lane) {
  ChP r; const int o = ch * 64 + lane;
  r.p[0] = ((const u32*)a.are)[o]; r.p[1] = ((const u32*)a.aim)[o]; r.p[2] = ((const u32*)a.bre)[o];
  r.p[3] = ((const u32*)a.bim)[o]; r.p[4] = ((const u32*)a.cre)[o]; r.p[5] = ((const u32*)a.cim)[o];
  r.l = a.ldt[ch];
  return r;
}

constexpr int XY_STRIDE = 528, CP_STRIDE = 4160;
typedef u32 u32x8 __attribute__((ext_vector_type(8)));
typedef u32 u32x16 __attribute__((ext_vector_type(16)));

DI float sum8(float v) {
  v += __builtin_bit_cast(float, __builtin_amdgcn_update_dpp(0, __builtin_bit_cast(int, v), 0xB1, 0xf, 0xf, true));
  v += __builtin_bit_cast(float, __builtin_amdgcn_update_dpp(0, __builtin_bit_cast(int, v), 0x4E, 0xf, 0xf, true));
  v += __builtin_bit_cast(float, __builtin_amdgcn_update_dpp(0, __builtin_bit_cast(int, v), 0x141, 0xf, 0xf, true));
  return v;
}

DI void ssm_block(const SsmArgs& a, int c, LAS char* sm) {

  const int tid = threadIdx.x, lane = tid & 63;
  const int w = __builtin_amdgcn_readfirstlane(tid >> 6);
  GAS u32* ctl = (GAS u32*)(a.ws + WS_CTL);
  const int r8 = lane >> 3, j8 = lane & 7, row = 32 * c + 8 * w + r8;
  u32x4 v[9];
  #pragma unroll
  for (int k = 0; k < 9; ++k) v[k] = *(const u32x4*)(a.x + (size_t)row * D_ + (j8 + 8 * k) * 8);
  const u32 ep = RLX_LD(ctl);
  const ChP p0 = load_ch(a, 9 * c + w, lane), p1 = load_ch(a, 9 * c + w + 4, lane), p2 = load_ch(a, 9 * c + 8, lane);
  const u32 d0 = a.dsk[9 * c + w], d1 = a.dsk[9 * c + w + 4], d2 = a.dsk[9 * c + 8];
  {
    const u32 vo = (((c & 7) * 4 + w) * 16 + (lane & 15)) * 128;
    asm volatile("s_getpc_b64 s[40:41]\n Lpc_%=:\n s_add_u32 s40, s40, ssmffn_text_begin-Lpc_%=\n s_addc_u32 s41, s41, (ssmffn_text_begin-Lpc_%=)>>32\n s_and_b32 s41, s41, 0xffff\n"
                 "s_mov_b32 s42, ssmffn_text_end-ssmffn_text_begin\n s_max_i32 s42, s42, 0\n s_mov_b32 s43, 0x20000\n s_nop 1\n"
                 "buffer_load_dword a250, %0, s[40:43], 0 offen" :: "v"(vo) : "s40", "s41", "s42", "s43", "scc", "a250");
  }
  {
    const int wi = (c & 7) * 4 + w;
    auto rw = MKRSRC((const char*)a.w1 + (c >> 4) * 2304 + (size_t)wi * 18 * 9216, 18 * 9216);
    const int rr = lane / 3, sp = lane - 3 * rr;
    #pragma unroll
    for (int q = 0; q < 2; ++q) pf_line(rw, rr * 9216 + sp * 768, q * 128);
  }
  const u32 wb = (u32)(size_t)sm + w * W_BYTES;
  const int r16 = lane & 15, g4 = lane >> 4, cp = (3 - r16) & 3;
  if (tid < 4) *(LAS a_u32*)(sm + SH_FL + 4 * tid) = 0;
  if (tid == 4) *(LAS a_u32*)(sm + SH_KT + 1536) = 0;
  u32x16 ca, cb;
  {
    const u32 kt = wb + W_KT;
    ca[0] = kt + 2 * (128 * (g4 >> 1) + 4 * (g4 & 1) + 8 * r16);
    ca[1] = kt + 2 * (g4 < 2 ? 256 + 64 * g4 + 4 * r16 : 256 + 64 * g4 + 2 * r16);
    ca[2] = ca[1] + (g4 < 2 ? 4 : 64);
    ca[3] = kt + 2 * (512 + 64 * g4 + r16);
    ca[4] = kt + 2 * (384 + 191 - lane);
    ca[5] = kt + 2 * (256 + 2 * (127 - lane));
    ca[6] = lane == 63 ? kt + 2 * 511 : ca[4];
    ca[7] = lane == 63 ? kt + 2 * 383 : ca[5];
    ca[8] = lane == 63 ? kt + 2 * 382 : ca[5] - 2;
    ca[9] = (lane & 1) ? 0x05040100u : 0x01000100u;
    #pragma unroll
    for (int i = 0; i < 11; ++i) {
      const int lag = 511 - 8 * lane - i;
      const u32 v_ = kt + (lag < 0 ? 1536 : lag >= 256 ? 2 * (128 + ((lag + ((lag >> 1) & 1)) >> 1)) : 2 * lag);
      if (i < 6) ca[10 + i] = v_; else cb[i - 6] = v_;
    }
    cb[5] = wb + W_CP + cp * CP_STRIDE + 2 * (2055 - r16 - cp - 8 * g4) - 3584 - 64;
    cb[6] = wb + W_U + 2 * (256 + 16 * r16 - 8 * g4 + 8) - 64;
    cb[7] = 512 * (g4 & 1) + 32 * r16 + 16 * (g4 >> 1);
    cb[8] = wb + W_ST + lane * 8;
    cb[9] = wb + W_ST + r16 * XY_STRIDE + g4 * 16;
    cb[10] = wb + W_CP + lane * 16;
    cb[11] = (lane >> 2) * 36864 + (lane & 3) * 16; cb[12] = (lane / 3) * 9216 + (lane % 3) * 768; cb[13] = 0;
    cb[14] = lane * 16; cb[15] = lane * 1152;
  }
  asm volatile("" : "+v"(ca), "+v"(cb));

  u32 tag;
  {
    float s0 = 0.f, s1 = 0.f;
    #pragma unroll
    for (int k = 0; k < 9; ++k) { s0 = dot2b(v[k][0], 0x3f803f80u, s0); s1 = dot2b(v[k][1], 0x3f803f80u, s1); s0 = dot2b(v[k][2], 0x3f803f80u, s0); s1 = dot2b(v[k][3], 0x3f803f80u, s1); }
    float s = s0 + s1;
    s = sum8(s);
    const float mean = s * (1.0f / 576.0f), m = rbf(mean);
    u32 clo = 0x00003f80u, chi = 0x3f800000u;
    asm volatile("" : "+s"(clo), "+s"(chi));
    f32x2 t[36]; f32x2 q0 = {0.f, 0.f}, q1 = {0.f, 0.f};
    const float nm = -m;
    #pragma unroll
    for (int k = 0; k < 9; ++k)
      asm volatile("v_dot2_f32_bf16 %0, %8, %12, %14\n v_dot2_f32_bf16 %1, %8, %13, %14\n v_dot2_f32_bf16 %2, %9, %12, %14\n v_dot2_f32_bf16 %3, %9, %13, %14\n"
                   "v_dot2_f32_bf16 %4, %10, %12, %14\n v_dot2_f32_bf16 %5, %10, %13, %14\n v_dot2_f32_bf16 %6, %11, %12, %14\n v_dot2_f32_bf16 %7, %11, %13, %14\n s_nop 2"
                   : "=&v"(t[4 * k].x), "=&v"(t[4 * k].y), "=&v"(t[4 * k + 1].x), "=&v"(t[4 * k + 1].y), "=&v"(t[4 * k + 2].x), "=&v"(t[4 * k + 2].y), "=&v"(t[4 * k + 3].x), "=&v"(t[4 * k + 3].y)
                   : "v"(v[k][0]), "v"(v[k][1]), "v"(v[k][2]), "v"(v[k][3]), "s"(clo), "s"(chi), "v"(nm));
    #pragma unroll
    for (int k = 0; k < 36; ++k) { if (k & 1) q1 = __builtin_elementwise_fma(t[k], t[k], q1); else q0 = __builtin_elementwise_fma(t[k], t[k], q0); }
    float q = (q0.x + q0.y) + (q1.x + q1.y);
    q = sum8(q);
    const float dm = mean - m;
    const float var = fmaxf(q * (1.0f / 576.0f) - dm * dm, 0.f);
    const float den = rbf(__builtin_amdgcn_sqrtf(rbf(rbf(var) + 1e-5f)));
    const float rs = __builtin_amdgcn_rcpf(den);
    if (j8 == 0) RLX_ST((GAS float*)(a.ws + WS_RSTD) + row, rs);
    #pragma unroll
    for (int k = 0; k < 9; ++k) {
      u32x4 o;
      #pragma unroll
      for (int e = 0; e < 4; ++e) { const f32x2 y = t[4 * k + e] * rs; o[e] = pk_bf(y.x, y.y); }
      *(LAS a_u32x4*)(sm + (8 * w + r8) * 1168 + (j8 + 8 * k) * 16) = o;
    }

    __syncthreads();
    u32x4 tv[9];
    #pragma unroll
    for (int g = 0; g < 9; ++g) {
      LAS char* p = sm + (8 * (r16 & 3) + (r16 >> 2)) * 1168 + (144 * w + 16 * g + 4 * g4) * 2;
      const u32x2 l2 = __builtin_bit_cast(u32x2, __builtin_amdgcn_ds_read_tr16_b64_v4i16((LAS s16x4*)p));
      const u32x2 h2 = __builtin_bit_cast(u32x2, __builtin_amdgcn_ds_read_tr16_b64_v4i16((LAS s16x4*)(p + 4 * 1168)));
      tv[g] = u32x4{l2.x, l2.y, h2.x, h2.y};
    }
    #pragma unroll
    for (int k = 0; k < 9; ++k)
      __builtin_amdgcn_raw_buffer_store_b128(tv[k], MKRSRC(a.ws + WS_XNT, 2359296), (4 * g4 + (r16 & 3)) * 64 + (r16 >> 2) * 16, c * 36864 + (144 * w + 16 * k) * 64, 16);
    asm volatile("s_nop 1" :: "v"(tv[8]), "v"(tv[7]));
    tag = __builtin_amdgcn_readfirstlane(ep) + 1u;

    asm volatile("s_waitcnt lgkmcnt(0)\n s_barrier" ::: "memory");

  }
  if (lane == 0) *(LAS a_u32*)(sm + w * W_BYTES + W_KT + 1536) = 0;
  u32 ready;
  {
    const u32 m0_ = lane == 0 ? 0xffffffffu : 0u;
    u32x8 q0 = {p0.p[0], p0.p[1], p0.p[2], p0.p[3], p0.p[4], p0.p[5], p0.l, (d0 << 16) & m0_};
    u32x8 q1 = {p1.p[0], p1.p[1], p1.p[2], p1.p[3], p1.p[4], p1.p[5], p1.l, (d1 << 16) & m0_};
    u32x8 q2 = {p2.p[0], p2.p[1], p2.p[2], p2.p[3], p2.p[4], p2.p[5], p2.l, (d2 << 16) & m0_};
    const char* pfv = (const char*)a.w1 + (c >> 4) * 2304 + (size_t)((c & 7) * 4 + w) * 18 * 9216;
    const char* pfw = (const char*)a.w2 + (size_t)(c >> 4) * 1152 * 1152 + (size_t)((c & 7) * 4 + w) * 36 * 1152;
    asm volatile(
      "s_mov_b32 s40, %6\n s_mov_b32 s41, %7\n s_mov_b64 s[44:45], %8\n s_mov_b64 s[48:49], %9\n s_mov_b64 s[52:53], %10\n s_mov_b32 s54, %11\n"
      "s_mov_b32 s56, %12\n s_mul_i32 s55, s56, 9\n s_mov_b32 s57, %13\n s_mov_b64 s[60:61], %14\n s_mov_b64 s[92:93], %15\n"
      SSM_BODY

      "s_mov_b32 %0, s67\n"
      : "=s"(ready) : "{v[48:63]}"(ca), "{v[64:79]}"(cb), "{v[80:87]}"(q0), "{v[88:95]}"(q1), "{v[96:103]}"(q2),
         "s"(w), "s"(wb), "s"(a.ws + WS_XNT), "s"(a.ws + WS_AT), "s"(ctl), "s"(tag), "s"(c), "s"(blockIdx.x), "s"(pfw), "s"(pfv)
      : "memory", "scc", "vcc", CLOB_SSM);
  }
  __builtin_amdgcn_s_barrier();
  const int lane2 = __builtin_amdgcn_mbcnt_hi(-1, __builtin_amdgcn_mbcnt_lo(-1, 0));
  #define lane lane2
  if (w == 0 && lane == 0) RLX_ST(ctl + C_SD + c, tag);
  const int i = c >> 2, hb0 = ((w >> 1) * 4 + (c & 3)) * 2 + (w & 1);
  GAS u32* pfa = ctl + C_FA + 32 + 512 * (i * 12 + (lane < 12 ? lane : 0));
  LAS char* s3 = sm + w * 4096;
  Piece P0, P1, P2, P3;
  if (!(ready & 1)) poll_lv<4>(pfa + 32 * 2, tag, lane);

  piece_load_part(P0, a.ws, i, hb0, lane); piece_load_x(P0, a.x, a.ws, i, hb0, lane);
  piece_load_part(P1, a.ws, i, hb0 + 16, lane); piece_load_x(P1, a.x, a.ws, i, hb0 + 16, lane);
  const u32 sd = RLX_LD(ctl + C_SD + lane);
  if (!(ready & 2)) poll_lv<4>(pfa + 32 * 4, tag, lane);
  if (!__all(sd == tag)) poll_eq<8>(ctl + C_SD + lane, tag);
  __builtin_amdgcn_fence(__ATOMIC_ACQUIRE, "wavefront");

  piece_load_a(P0, a.ws, i, hb0, lane); piece_load_a(P1, a.ws, i, hb0 + 16, lane);
  piece_load_part(P2, a.ws, i, hb0 + 32, lane); piece_load_side(P2, a.x, a.ws, i, hb0 + 32, lane);
  piece_load_part(P3, a.ws, i, hb0 + 48, lane); piece_load_side(P3, a.x, a.ws, i, hb0 + 48, lane);
  piece_finish(P0, a.out, i, hb0, lane, s3);
  piece_finish(P1, a.out, i, hb0 + 16, lane, s3 + 1024);
  piece_finish(P2, a.out, i, hb0 + 32, lane, s3 + 2048);
  piece_finish(P3, a.out, i, hb0 + 48, lane, s3 + 3072);

  if (c == 0 && w == 1) {
    #pragma unroll
    for (int i = 0; i < 4; ++i) poll_eq<16>(ctl + C_ACK + 64 * i + lane, tag);
    if (lane == 0) RLX_ST(ctl, tag);
  }
  asm volatile("ssmffn_text_end:");
  #undef lane
}

__global__ __launch_bounds__(256, 1) void main_k(const u16* x, const u16* are, const u16* aim, const u16* bre, const u16* bim, const u16* cre, const u16* cim,
                                                 const u16* ldt, const u16* dsk, const u16* w1, const u16* w2, char* ws, u16* out) {
  extern __shared__ __attribute__((aligned(16))) char smem[];
  const int b = blockIdx.x, g = b & 7, j = ((b >> 3) + 24) & 31;
  if (j >= 24) {

    SsmArgs a = {x, are, aim, bre, bim, cre, cim, ldt, dsk, w1, w2, ws, out};
    ssm_block(a, g * 8 + (j - 24), (LAS char*)smem);

    return;
  }

  const int i = (g & 1) * 8 + (j & 7), s = (g >> 1) * 3 + (j >> 3);
  const u32 w = __builtin_amdgcn_readfirstlane(threadIdx.x >> 6);
  GAS u32* ctl = (GAS u32*)(ws + WS_CTL);
  u32 tag, ready;
  {
    const char* xp = (const char*)x + (size_t)(128 * i + 32 * w) * 1152;
    asm volatile("s_mov_b64 s[40:41], %0\n s_mov_b32 s45, %1\n" FFN_EARLY :: "s"(xp), "s"(w)
                 : "memory", "scc", "s40", "s41", "s42", "s43", "s44", "s45", "s46", "v0", "v1", "v2", "v3", "v11", "v13", "v48");
    __builtin_amdgcn_sched_barrier(0);
    const char* w1p = (const char*)w1 + s * 768;
    const char* w2p = (const char*)w2 + (size_t)s * 384 * 1152;
    char* pp = ws + WS_PART + (size_t)i * 18 * 4 * 12 * 2048 + (size_t)(w * 12 + s) * 2048;
    const u32 w1n = 576 * 9216 - s * 768;
    const u32 fa = (C_FA + 512 * (i * 12 + s)) * 4, ack = (C_ACK + b) * 4;
    asm volatile(
      "ssmffn_text_begin:\n"

      "s_mov_b64 s[40:41], %2\n s_mov_b64 s[44:45], %3\n s_mov_b64 s[48:49], %4\n s_mov_b64 s[52:53], %5\n s_mov_b32 s46, %6\n s_mov_b32 s61, %7\n"
      "s_mov_b64 s[84:85], %8\n s_mov_b32 s86, %9\n s_mov_b32 s87, %10\n s_mov_b32 s91, %11\n"
      FFN_BODY

      "s_mov_b32 %0, s88\n s_mov_b32 %1, s95\n"
      : "=&s"(tag), "=&s"(ready) : "s"(xp), "s"(w1p), "s"(w2p), "s"(pp), "s"(w1n), "s"(w), "s"(ctl), "s"(fa), "s"(ack), "s"(s)
      : "memory", "scc", "vcc", CLOB_ALL);
  }
  const int lane = __builtin_amdgcn_mbcnt_hi(-1, __builtin_amdgcn_mbcnt_lo(-1, 0));
  GAS u32* psd = ctl + C_SD + lane;
  GAS u32* pfa = ctl + C_FA + 32 + 512 * (i * 12 + (lane < 12 ? lane : 0));
  __builtin_amdgcn_s_barrier();
  if (w == 0 && lane == 0) RLX_ST(ctl + C_FA + 512 * (i * 12 + s) + 32 * 10, tag);
  const int hb = (12 + 2 * (s >> 2) + (w & 1)) * 8 + 2 * (s & 3) + (w >> 1), need = s < 4 ? 7 : 9;

  if (w == 3 && !ready) {
    poll_eq<8>(psd, tag);
    poll_lv<4>(pfa + 32 * 6, tag, lane);
  }
  asm volatile("s_barrier" ::: "memory");

  Piece P, Q;
  const int pm = w < 2 ? s : 12 + (s >> 1), tm = w < 2 ? w : s & 1;
  const int hm = (8 + 2 * (pm >> 3) + tm) * 8 + (pm & 7);
  const bool mid = w < 2 || (w == 2 && s < 8);
  if (mid) { piece_load_part(Q, ws, i, hm, lane); piece_load_side(Q, x, ws, i, hm, lane); }
  piece_load_side(P, x, ws, i, hb, lane);
  if (w == 3) poll_lv<1>(pfa + 32 * need, tag, lane);
  asm volatile("s_barrier" ::: "memory");

  piece_load_part(P, ws, i, hb, lane);
  if (mid) piece_finish(Q, out, i, hm, lane, (LAS char*)smem + w * 4096 + 1024);
  piece_finish(P, out, i, hb, lane, (LAS char*)smem + w * 4096);


}

extern "C" void kernel_launch(void* const* d_in, const int* in_sizes, int n_in, void* d_out, int out_size, void* d_ws, size_t ws_size, hipStream_t stream) {
  (void)hipFuncSetAttribute((const void*)main_k, hipFuncAttributeMaxDynamicSharedMemorySize, LDS_BYTES);
  char* ws = (char*)d_ws;
  main_k<<<256, 256, LDS_BYTES, stream>>>((const u16*)d_in[0], (const u16*)d_in[1], (const u16*)d_in[2], (const u16*)d_in[3], (const u16*)d_in[4], (const u16*)d_in[5],
                                          (const u16*)d_in[6], (const u16*)d_in[7], (const u16*)d_in[8], (const u16*)d_in[10], (const u16*)d_in[11], ws, (u16*)d_out);
}
```
